```python
import torch

def _softmax(x, axis=-1):
    return torch.softmax(x, dim=axis)
def _gelu(x):
    return torch.nn.functional.gelu(x, approximate="tanh")
def _silu(x):
    return torch.nn.functional.silu(x)
def _sigmoid(x):
    return torch.sigmoid(x)
def _layernorm(x, axis=-1, eps=1e-5):
    m = x.mean(dim=axis, keepdim=True)
    v = x.var(dim=axis, keepdim=True, unbiased=False)
    return (x - m) / torch.sqrt(v + eps)
def _scatter_add(tgt, idx, src, axis=0):
    return tgt.clone().index_add_(axis, idx, src)
def _segment_sum(src, seg, n):
    out = torch.zeros((n,) + src.shape[1:], dtype=src.dtype)
    return out.index_add_(0, seg, src)
def _pad(x, pw):
    flat = tuple(v for pair in reversed(pw) for v in pair)
    return torch.nn.functional.pad(x, flat)

kernel_name = 'standalone__weight_only_int_quip__prompt'

def setup_inputs():

    _w = (lambda a: (lambda t: t.to(torch.bfloat16) if t.is_floating_point() else t)(torch.as_tensor(a)))
    B, S, D, F = 1, 2048, 512, 1376
    G = 128 if 128 > 0 else D
    D = (D // G) * G
    ng = D // G
    bits = 4 if 4 > 0 else 4
    qmax = 1 << bits
    qw = torch.randint(0, qmax, (D, F)).to(torch.int32)
    U, _ = (lambda *_a, **_k: torch.linalg.qr(*[torch.as_tensor(_x).float() for _x in _a], **_k))((lambda *s: torch.randn(*s, dtype=torch.bfloat16) * 0.05)(D, D))
    V, _ = (lambda *_a, **_k: torch.linalg.qr(*[torch.as_tensor(_x).float() for _x in _a], **_k))((lambda *s: torch.randn(*s, dtype=torch.bfloat16) * 0.05)(F, F))
    return {
        "x": _w((lambda *s: torch.randn(*s, dtype=torch.bfloat16) * 0.05)(B, S, D)),
        "qw": _w(qw),
        "scales": _w(torch.abs((lambda *s: torch.randn(*s, dtype=torch.bfloat16) * 0.05)(ng, F)) * 0.02 + 0.005),
        "zeros": _w(torch.full((ng, F), qmax / 2.0, dtype=float)),
        "U": _w(U.to(float)),
        "V": _w(V.to(float)),
    }

def reference(x, qw, scales, zeros, U, V):
    B, S, D = x.shape
    ng, F = scales.shape
    G = D // ng
    s = (lambda x, n, axis: torch.repeat_interleave(x, n, dim=axis))(scales, G, axis=0)
    z = (lambda x, n, axis: torch.repeat_interleave(x, n, dim=axis))(zeros, G, axis=0)
    wq = ((lambda x, dt: x.to(dt))(qw, x.dtype) - z) * s
    xr = torch.einsum("bsd,de->bse", x, U)
    h  = torch.einsum("bse,ef->bsf", xr, wq)
    y  = torch.einsum("bsf,fg->bsg", h, V)
    return y
```

```cpp
#include <hip/hip_runtime.h>
#include <hip/hip_bf16.h>
#include <stdint.h>

typedef __attribute__((ext_vector_type(8))) short bf16x8;
typedef __attribute__((ext_vector_type(4))) float f32x4;
typedef unsigned short u16;

__device__ __forceinline__ float bf2f(u16 v) { return __uint_as_float(((unsigned)v) << 16); }
__device__ __forceinline__ u16 f2bf(float f) {
  __hip_bfloat16 h = __float2bfloat16(f);
  return *reinterpret_cast<u16*>(&h);
}

template <bool DEQ>
__device__ void gemm_tile(const void* Av, const u16* sc, const u16* zr, const u16* B, u16* C,
                          int M, int N, int K, int bm, int bn, u16* As, u16* Bs) {
  const int tid = threadIdx.x, lane = tid & 63, w = tid >> 6;
  const int wr = w >> 1, wc = w & 1;
  f32x4 acc[2][2] = {};
  for (int k0 = 0; k0 < K; k0 += 32) {
    for (int i = tid; i < 64 * 32; i += 256) {
      int r = i >> 5, c = i & 31;
      int gr = bm + r, gk = k0 + c;
      u16 v;
      if (DEQ) {
        int q = ((const int*)Av)[(size_t)gr * K + gk];
        int g = gr >> 7;
        float s = bf2f(sc[g * K + gk]), z = bf2f(zr[g * K + gk]);
        v = f2bf(bf2f(f2bf((float)q - z)) * s);
      } else {
        v = ((const u16*)Av)[(size_t)gr * K + gk];
      }
      As[r * 40 + c] = v;
    }
    for (int i = tid; i < 64 * 32; i += 256) {
      int kk = i >> 6, c = i & 63;
      int gc = bn + c;
      u16 v = gc < N ? B[(size_t)(k0 + kk) * N + gc] : 0;
      Bs[c * 40 + kk] = v;
    }
    __syncthreads();
    bf16x8 a[2], b[2];
    for (int m = 0; m < 2; ++m) a[m] = *(const bf16x8*)&As[(wr * 32 + m * 16 + (lane & 15)) * 40 + (lane >> 4) * 8];
    for (int n = 0; n < 2; ++n) b[n] = *(const bf16x8*)&Bs[(wc * 32 + n * 16 + (lane & 15)) * 40 + (lane >> 4) * 8];
    for (int m = 0; m < 2; ++m)
      for (int n = 0; n < 2; ++n)
        acc[m][n] = __builtin_amdgcn_mfma_f32_16x16x32_bf16(a[m], b[n], acc[m][n], 0, 0, 0);
    __syncthreads();
  }
  for (int m = 0; m < 2; ++m)
    for (int n = 0; n < 2; ++n)
      for (int j = 0; j < 4; ++j) {
        int r = bm + wr * 32 + m * 16 + (lane >> 4) * 4 + j;
        int c = bn + wc * 32 + n * 16 + (lane & 15);
        if (c < N) C[(size_t)r * N + c] = f2bf(acc[m][n][j]);
      }
}

__global__ __launch_bounds__(256) void k1(const u16* x, const int* qw, const u16* sc, const u16* zr,
                                          const u16* U, const u16* V, u16* xr, u16* T) {
  __shared__ __attribute__((aligned(16))) u16 As[64 * 40], Bs[64 * 40];
  int b = blockIdx.x;
  if (b < 8 * 22) {
    gemm_tile<true>(qw, sc, zr, V, T, 512, 1376, 1376, (b / 22) * 64, (b % 22) * 64, As, Bs);
  } else {
    b -= 8 * 22;
    gemm_tile<false>(x, nullptr, nullptr, U, xr, 2048, 512, 512, (b / 8) * 64, (b % 8) * 64, As, Bs);
  }
}
__global__ __launch_bounds__(256) void k2(const u16* xr, const u16* T, u16* y) {
  __shared__ __attribute__((aligned(16))) u16 As[64 * 40], Bs[64 * 40];
  int b = blockIdx.x;
  gemm_tile<false>(xr, nullptr, nullptr, T, y, 2048, 1376, 512, (b / 22) * 64, (b % 22) * 64, As, Bs);
}

extern "C" void kernel_launch(void* const* d_in, const int* in_sizes, int n_in,
                              void* d_out, int out_size, void* d_ws, size_t ws_size,
                              hipStream_t stream) {
  const u16* x = (const u16*)d_in[0];
  const int* qw = (const int*)d_in[1];
  const u16* sc = (const u16*)d_in[2];
  const u16* zr = (const u16*)d_in[3];
  const u16* U = (const u16*)d_in[4];
  const u16* V = (const u16*)d_in[5];
  u16* xr = (u16*)d_ws;
  u16* T = xr + 2048 * 512;
  k1<<<8 * 22 + 32 * 8, 256, 0, stream>>>(x, qw, sc, zr, U, V, xr, T);
  k2<<<32 * 22, 256, 0, stream>>>(xr, T, (u16*)d_out);
}
```

```cpp
#define PROBE 9
#define STAT 3
#include <hip/hip_runtime.h>
#include <hip/hip_bf16.h>
#include <stdint.h>

typedef __attribute__((ext_vector_type(8))) short bf16x8;
typedef __attribute__((ext_vector_type(4))) float f32x4;
typedef unsigned short u16;

__device__ __forceinline__ float bf2f(u16 v) { return __uint_as_float(((unsigned)v) << 16); }
__device__ __forceinline__ u16 f2bf(float f) {
  __hip_bfloat16 h = __float2bfloat16(f);
  return *reinterpret_cast<u16*>(&h);
}

template <bool DEQ>
__device__ void gemm_tile(const void* Av, const u16* sc, const u16* zr, const u16* B, u16* C,
                          int M, int N, int K, int bm, int bn, u16* As, u16* Bs) {
  const int tid = threadIdx.x, lane = tid & 63, w = tid >> 6;
  const int wr = w >> 1, wc = w & 1;
  f32x4 acc[2][2] = {};
  for (int k0 = 0; k0 < K; k0 += 32) {
    for (int i = tid; i < 64 * 32; i += 256) {
      int r = i >> 5, c = i & 31;
      int gr = bm + r, gk = k0 + c;
      u16 v;
      if (DEQ) {
        int q = ((const int*)Av)[(size_t)gr * K + gk];
        int g = gr >> 7;
        float s = bf2f(sc[g * K + gk]), z = bf2f(zr[g * K + gk]);
        v = f2bf(bf2f(f2bf((float)q - z)) * s);
      } else {
        v = ((const u16*)Av)[(size_t)gr * K + gk];
      }
      As[r * 40 + c] = v;
    }
    for (int i = tid; i < 64 * 32; i += 256) {
      int kk = i >> 6, c = i & 63;
      int gc = bn + c;
      u16 v = gc < N ? B[(size_t)(k0 + kk) * N + gc] : 0;
      Bs[c * 40 + kk] = v;
    }
    __syncthreads();
    bf16x8 a[2], b[2];
    for (int m = 0; m < 2; ++m) a[m] = *(const bf16x8*)&As[(wr * 32 + m * 16 + (lane & 15)) * 40 + (lane >> 4) * 8];
    for (int n = 0; n < 2; ++n) b[n] = *(const bf16x8*)&Bs[(wc * 32 + n * 16 + (lane & 15)) * 40 + (lane >> 4) * 8];
    for (int m = 0; m < 2; ++m)
      for (int n = 0; n < 2; ++n)
        acc[m][n] = __builtin_amdgcn_mfma_f32_16x16x32_bf16(a[m], b[n], acc[m][n], 0, 0, 0);
    __syncthreads();
  }
  for (int m = 0; m < 2; ++m)
    for (int n = 0; n < 2; ++n)
      for (int j = 0; j < 4; ++j) {
        int r = bm + wr * 32 + m * 16 + (lane >> 4) * 4 + j;
        int c = bn + wc * 32 + n * 16 + (lane & 15);
        if (c < N) C[(size_t)r * N + c] = f2bf(acc[m][n][j]);
      }
}

__global__ __launch_bounds__(256) void k1(const u16* x, const int* qw, const u16* sc, const u16* zr,
                                          const u16* U, const u16* V, u16* xr, u16* T) {
  __shared__ __attribute__((aligned(16))) u16 As[64 * 40], Bs[64 * 40];
  int b = blockIdx.x;
  if (b < 8 * 22) {
    gemm_tile<true>(qw, sc, zr, V, T, 512, 1376, 1376, (b / 22) * 64, (b % 22) * 64, As, Bs);
  } else {
    b -= 8 * 22;
    gemm_tile<false>(x, nullptr, nullptr, U, xr, 2048, 512, 512, (b / 8) * 64, (b % 8) * 64, As, Bs);
  }
}
__global__ __launch_bounds__(256) void k2(const u16* xr, const u16* T, u16* y) {
  __shared__ __attribute__((aligned(16))) u16 As[64 * 40], Bs[64 * 40];
  int b = blockIdx.x;
  gemm_tile<false>(xr, nullptr, nullptr, T, y, 2048, 1376, 512, (b / 22) * 64, (b % 22) * 64, As, Bs);
}

typedef unsigned v4u __attribute__((ext_vector_type(4)));
__device__ __forceinline__ unsigned long long rt() { return __builtin_amdgcn_s_memrealtime(); }
#define GLDS(g, l) __builtin_amdgcn_global_load_lds((const __attribute__((address_space(1))) void*)(g), (__attribute__((address_space(3))) void*)(l), 16, 0, 0)
__global__ __launch_bounds__(512) void probe(const char* x, const char* qw, const char* U, const char* V, unsigned long long* st) {
  extern __shared__ __attribute__((aligned(16))) char lds[];
  unsigned long long t0 = rt();
  unsigned long long c0 = __builtin_amdgcn_s_memtime();
  int b = blockIdx.x;
  int tid = threadIdx.x, w = __builtin_amdgcn_readfirstlane(tid >> 6), lane = tid & 63;
  __syncthreads();
  unsigned long long t2 = rt();
  unsigned long long c2 = __builtin_amdgcn_s_memtime();
  if (tid == 0) { st[b * 4] = t0; st[b * 4 + 1] = c2 - c0; st[b * 4 + 2] = t2; }
}
__global__ __launch_bounds__(256) void kdelay(const unsigned long long* st) {
  __shared__ unsigned long long s0[256], s2[256], sc[256];
  int t = threadIdx.x;
  unsigned long long tb = rt();
  s0[t] = st[t * 4]; sc[t] = st[t * 4 + 1]; s2[t] = st[t * 4 + 2];
  __syncthreads();
  __shared__ unsigned long long medv;
  { int c = 0; for (int j = 0; j < 256; ++j) c += (sc[j] < sc[t]) || (sc[j] == sc[t] && j < t); if (c == 128) medv = sc[t]; }
  __syncthreads();
  if (t == 0) {
    unsigned long long mn = ~0ull, mx = 0, mxs = 0;
    for (int i = 0; i < 256; ++i) { mn = s0[i] < mn ? s0[i] : mn; mx = s2[i] > mx ? s2[i] : mx; mxs = s0[i] > mxs ? s0[i] : mxs; }
    unsigned long long val;
    if (STAT == 0) {
      unsigned long long med = medv;
      val = med * 4;
    } else if (STAT == 1) val = (mx - mn) * 100;
    else if (STAT == 2) val = (mxs - mn) * 100;
    else val = 0;
    if (val > 200000) val = 200000;
    val += 50000;
    while (rt() - mn < val && rt() - tb < 400000) __builtin_amdgcn_s_sleep(2);
  }
}
extern "C" void kernel_launch(void* const* d_in, const int* in_sizes, int n_in,
                              void* d_out, int out_size, void* d_ws, size_t ws_size,
                              hipStream_t stream) {
  const u16* x = (const u16*)d_in[0];
  const int* qw = (const int*)d_in[1];
  const u16* sc = (const u16*)d_in[2];
  const u16* zr = (const u16*)d_in[3];
  const u16* U = (const u16*)d_in[4];
  const u16* V = (const u16*)d_in[5];
  u16* xr = (u16*)d_ws;
  u16* T = xr + 2048 * 512;
  unsigned long long* st = (unsigned long long*)((char*)d_ws + (32 << 20));
  (void)hipFuncSetAttribute((const void*)probe, hipFuncAttributeMaxDynamicSharedMemorySize, 128 * 1024);
  probe<<<256, 64, 0, stream>>>((const char*)x, (const char*)qw, (const char*)U, (const char*)V, st);
  k1<<<8 * 22 + 32 * 8, 256, 0, stream>>>(x, qw, sc, zr, U, V, xr, T);
  k2<<<32 * 22, 256, 0, stream>>>(xr, T, (u16*)d_out);
  kdelay<<<1, 256, 0, stream>>>(st);
}
```

```cpp
#include <hip/hip_runtime.h>
#include <stdint.h>

typedef unsigned u32;
typedef unsigned long long u64;
typedef unsigned short u16;
typedef u32 v4u __attribute__((ext_vector_type(4)));
typedef u32 v2u __attribute__((ext_vector_type(2)));
typedef float v4f __attribute__((ext_vector_type(4)));
typedef float v16f __attribute__((ext_vector_type(16)));
typedef __bf16 v8bf __attribute__((ext_vector_type(8)));
typedef __bf16 v2bf __attribute__((ext_vector_type(2)));
typedef float v2f __attribute__((ext_vector_type(2)));
typedef short v4s __attribute__((ext_vector_type(4)));
#define LAS __attribute__((address_space(3)))
#define CAS __attribute__((address_space(4)))
typedef __amdgpu_buffer_rsrc_t rsrc_t;

#define PHASES 7

constexpr int OFF_QF = 4096, OFF_XF = 8192, OFF_TF = 12288;
constexpr int OFF_QP = 65536, OFF_XR = 1 << 20, OFF_T = 4 << 20, WS_BYTES = 6 << 20;
constexpr int SC1 = 16;
constexpr int OOB = 0x40000000;
constexpr int L_RED = 98304, L_CB = 114688, L_EP = 98304, L_CNT = 131072, LDS_BYTES = 131072 + 64;
constexpr u32 SPIN_MAX = 1u << 16;

__device__ __forceinline__ rsrc_t mkrs(const void* p, int bytes) {
  return __builtin_amdgcn_make_buffer_rsrc((void*)p, (short)0, bytes, 0x00020000);
}
__device__ __forceinline__ v16f mfma32(v4u a, v4u b, v16f c) {
  return __builtin_amdgcn_mfma_f32_32x32x16_bf16(__builtin_bit_cast(v8bf, a), __builtin_bit_cast(v8bf, b), c, 0, 0, 0);
}
__device__ __forceinline__ u32 cvtpk(float a, float b) {
  v2f f = {a, b};
  return __builtin_bit_cast(u32, __builtin_convertvector(f, v2bf));
}
__device__ __forceinline__ float flo(u32 v) { return __builtin_bit_cast(float, v << 16); }
__device__ __forceinline__ float fhi(u32 v) { return __builtin_bit_cast(float, v & 0xffff0000u); }
__device__ __forceinline__ void bar() { asm volatile("s_barrier" ::: "memory"); }
__device__ __forceinline__ void bar_lgkm() { asm volatile("s_waitcnt lgkmcnt(0)\n\ts_barrier" ::: "memory"); }
__device__ __forceinline__ void drain() { asm volatile("s_waitcnt vmcnt(0)" ::: "memory"); }
template <int N> __device__ __forceinline__ void wvm() { asm volatile("s_waitcnt vmcnt(%0)" ::"n"(N) : "memory"); }
__device__ __forceinline__ v4u lds128(LAS char* p) { return *(LAS v4u*)p; }

__device__ __forceinline__ void poll(rsrc_t rw, int off, bool act, u32 tlo, u32 thi) {
  for (u32 it = 0; it < SPIN_MAX; ++it) {
    v2u f = __builtin_amdgcn_raw_buffer_load_b64(rw, off, 0, SC1);
    bool ok = !act || (f.x == tlo && f.y == thi);
    if (__builtin_amdgcn_ballot_w64(!ok) == 0) break;
    asm volatile("" ::: "memory");
  }
  __builtin_amdgcn_fence(__ATOMIC_ACQUIRE, "wavefront");
  asm volatile("" ::: "memory");
}

#define ST(i)

__device__ __forceinline__ u32 qxor1(u32 v) { return __builtin_amdgcn_mov_dpp(v, 0xB1, 0xf, 0xf, true); }
__device__ __forceinline__ u32 qxor2(u32 v) { return __builtin_amdgcn_mov_dpp(v, 0x4E, 0xf, 0xf, true); }

__device__ __forceinline__ void pack_unit(const int* qw, rsrc_t rw, int u, int l) {
  rsrc_t rq = mkrs(qw, 512 * 5504);
  int rt = u / 22, un = u - rt * 22;
  int voff = (rt * 32 + (l & 28)) * 5504 + un * 256 + (l >> 5) * 128 + (l & 3) * 16;
  if (un == 21 && l >= 32) voff = OOB;
  v4u a[4], b[4];
#pragma unroll
  for (int r = 0; r < 4; ++r) {
    a[r] = __builtin_amdgcn_raw_buffer_load_b128(rq, voff, r * 5504, 0);
    b[r] = __builtin_amdgcn_raw_buffer_load_b128(rq, voff + 64, r * 5504, 0);
  }
  u32 D[4];
#pragma unroll
  for (int r = 0; r < 4; ++r)
    D[r] = a[r].x | (a[r].z << 4) | (b[r].x << 8) | (b[r].z << 12) | (a[r].y << 16) | (a[r].w << 20) | (b[r].y << 24) | (b[r].w << 28);
  bool o1 = l & 1, o2 = l & 2;
  u32 x0 = qxor1(D[0]), x1 = qxor1(D[1]), x2 = qxor1(D[2]), x3 = qxor1(D[3]);
  u32 E0 = o1 ? x1 : D[0], E1 = o1 ? D[1] : x0;
  u32 E2 = o1 ? x3 : D[2], E3 = o1 ? D[3] : x2;
  u32 y0 = qxor2(E0), y1 = qxor2(E1), y2 = qxor2(E2), y3 = qxor2(E3);
  u32 G0 = o2 ? y2 : E0, G2 = o2 ? E2 : y0;
  u32 G1 = o2 ? y3 : E1, G3 = o2 ? E3 : y1;
  u32 p0 = __builtin_amdgcn_perm(G1, G0, 0x06020400), p2 = __builtin_amdgcn_perm(G1, G0, 0x07030501);
  u32 p1 = __builtin_amdgcn_perm(G3, G2, 0x06020400), p3 = __builtin_amdgcn_perm(G3, G2, 0x07030501);
  auto s02 = __builtin_amdgcn_permlane32_swap(p0, p1, false, false);
  auto s13 = __builtin_amdgcn_permlane32_swap(p2, p3, false, false);
  v4u o = {s02[0], s13[0], s02[1], s13[1]};
  __builtin_amdgcn_raw_buffer_store_b128(o, rw, OFF_QP + u * 1024 + l * 16, 0, SC1);
}
__device__ __forceinline__ int qflag_off(int u) {
  int hh = u / 11;
  return OFF_QF + hh * 128 + (u - hh * 11) * 8;
}

template <int PH>
__global__ __launch_bounds__(512, 2) void kmain(const u16* __restrict__ x, const int* __restrict__ qw,
                                                const u16* __restrict__ sc, const u16* __restrict__ zr,
                                                const u16* __restrict__ U, const u16* __restrict__ V,
                                                u16* __restrict__ y, char* ws) {
  extern __shared__ __attribute__((aligned(16))) char smem_[];
  LAS char* smem = (LAS char*)smem_;
  ST(0);
  const int b = blockIdx.x, tid = threadIdx.x;
  const int w = __builtin_amdgcn_readfirstlane(tid >> 6), l = tid & 63;
  const int xcd = b & 7, r = b >> 3;
  const u64 tag = *(const CAS u64*)ws + 1;
  const u32 tlo = (u32)tag, thi = (u32)(tag >> 32);
  const v2u tagv = {tlo, thi};
  rsrc_t rw = mkrs(ws, WS_BYTES);
  if (tid == 0) *(LAS u32*)(smem + L_CNT) = 0;

  if (xcd < 2) {
    const int rp = xcd * 8 + (r >> 2), ep = r & 3;
    if (w >= 4) {
      const int lw = w - 4;
      rsrc_t rx = mkrs(x, 2048 * 1024), ru = mkrs(U, 512 * 1024);
      const int vx = (rp * 128 + lw * 32 + (l >> 3)) * 1024 + (((l & 7) ^ (l >> 4)) * 16);
      const int vu = (lw * 16 + (l >> 4)) * 1024 + ep * 256 + (((l & 15) ^ ((l >> 4) << 2)) * 16);
      auto issue = [&](int s) {
        LAS char* dst = smem + (s & 3) * 32768 + lw * 4096;
#pragma unroll
        for (int a = 0; a < 4; ++a) {
          __builtin_amdgcn_raw_ptr_buffer_load_lds(ru, (LAS void*)(dst + 16384 + a * 1024), 16, (vu ^ (a * 16)) + a * 4096, s * 65536, 0, 0);
          __builtin_amdgcn_raw_ptr_buffer_load_lds(rx, (LAS void*)(dst + a * 1024), 16, (vx ^ ((a & 1) * 64)) + a * 8192, s * 128, 0, 0);
        }
      };
      if (PH & 2) { issue(0); issue(1); issue(2); issue(3); }
      asm volatile("" ::"s"(tlo), "s"(thi));
      bar_lgkm();
      if (PH & 2) {
        wvm<24>(); bar();
        wvm<16>(); bar(); issue(4);
        wvm<16>(); bar(); issue(5);
        wvm<16>(); bar(); issue(6);
        wvm<16>(); bar(); issue(7);
        wvm<16>(); bar();
        wvm<8>(); bar();
        wvm<0>(); bar();
        bar();
      }
    } else {
      if ((PH & 1) && w == 3) { __builtin_amdgcn_s_setprio(3); pack_unit(qw, rw, 172 + xcd * 32 + r, l); }
      asm volatile("" ::"s"(tlo), "s"(thi));
      bar_lgkm();
      if ((PH & 1) && w == 3) {
        drain();
        if (l == 0) __builtin_amdgcn_raw_buffer_store_b64(tagv, rw, qflag_off(172 + xcd * 32 + r), 0, SC1);
        __builtin_amdgcn_s_setprio(0);
      }
      if (PH & 2) {
        const int we = w & 1, wm = w >> 1, h = l >> 5;
        const int rowb = wm * 64 + (l & 31);
        const int xb = rowb * 128 + ((h ^ ((rowb >> 1) & 7)) * 16);
        const int q = (l & 15) >> 2, p = l & 3, blk = (l >> 4) & 1;
        const int ub = 16384 + 256 * (8 * h + q) + 16 * ((((2 * we) ^ q) << 2) | ((2 * blk + (p >> 1)) ^ (2 * h))) + 8 * (p & 1);
        v16f acc[2][2] = {};
        v4u fa[2][2], fb[2][2];
        auto rd = [&](int n, int bi) {
          const int so = ((n >> 2) & 3) * 32768, ks = n & 3;
#pragma unroll
          for (int ce = 0; ce < 2; ++ce) {
            v2u t0 = __builtin_bit_cast(v2u, __builtin_amdgcn_ds_read_tr16_b64_v4i16((LAS v4s*)(smem + ((ub ^ (ce * 64)) + so + ks * 4096))));
            v2u t1 = __builtin_bit_cast(v2u, __builtin_amdgcn_ds_read_tr16_b64_v4i16((LAS v4s*)(smem + ((ub ^ (ce * 64) ^ 16) + so + ks * 4096 + 1024))));
            fa[bi][ce] = (v4u){t0.x, t0.y, t1.x, t1.y};
            fb[bi][ce] = lds128(smem + ((xb ^ (ks * 32)) + so + ce * 4096));
          }
        };
        bar();
        rd(0, 0);
#pragma unroll
        for (int n = 0; n < 32; ++n) {
          if (n + 1 < 32) {
            if (((n + 1) & 3) == 0) bar_lgkm();
            rd(n + 1, (n + 1) & 1);
          }
          __builtin_amdgcn_sched_barrier(0);
#pragma unroll
          for (int ce = 0; ce < 2; ++ce)
#pragma unroll
            for (int cm = 0; cm < 2; ++cm) acc[ce][cm] = mfma32(fa[n & 1][ce], fb[n & 1][cm], acc[ce][cm]);
          __builtin_amdgcn_sched_barrier(0);
        }
        bar();
        ST(1);
        const int ob = OFF_XR + (rp * 4 + wm * 2) * 32768 + (ep * 8 + we * 4) * 1024 + l * 16;
#pragma unroll
        for (int cm = 0; cm < 2; ++cm)
#pragma unroll
          for (int ce = 0; ce < 2; ++ce)
#pragma unroll
            for (int i = 0; i < 2; ++i) {
              v4u o;
#pragma unroll
              for (int t = 0; t < 4; ++t) o[t] = cvtpk(acc[ce][cm][8 * i + 2 * t], acc[ce][cm][8 * i + 2 * t + 1]);
              __builtin_amdgcn_raw_buffer_store_b128(o, rw, ob + (ce * 2 + i) * 1024, cm * 32768, SC1);
            }
        drain();
        if (l == 0) {
          u32 c = __hip_atomic_fetch_add((LAS u32*)(smem + L_CNT), 1u, __ATOMIC_RELAXED, __HIP_MEMORY_SCOPE_WORKGROUP);
          if (c == 3) __builtin_amdgcn_raw_buffer_store_b64(tagv, rw, OFF_XF + rp * 128 + ep * 8, 0, SC1);
        }
        ST(2);
      }
    }
  } else {
    const int xi = xcd - 2;
    const int rr = xi < 5 ? r - 4 : r;
    if (rr < 0) {
      const int u0 = 236 + (xi * 4 + r) * 6;
      if ((PH & 1) && w < 6 && u0 + w < 352) pack_unit(qw, rw, u0 + w, l);
      asm volatile("" ::"s"(tlo), "s"(thi));
      drain();
      bar_lgkm();
      if ((PH & 1) && w == 0 && l < 6 && u0 + l < 352) __builtin_amdgcn_raw_buffer_store_b64(tagv, rw, qflag_off(u0 + l), 0, SC1);
    } else {
      const int P = xi * 7 + (rr >> 2), g = rr & 3;
      if (w == 7) {
        if (PH & 1) { __builtin_amdgcn_s_setprio(3); pack_unit(qw, rw, P * 4 + g, l); }
        asm volatile("" ::"s"(tlo), "s"(thi));
        bar_lgkm();
        if (PH & 1) {
          drain();
          if (l == 0) __builtin_amdgcn_raw_buffer_store_b64(tagv, rw, qflag_off(P * 4 + g), 0, SC1);
          __builtin_amdgcn_s_setprio(0);
        }
      }
      if (PH & 2) {
        rsrc_t rv = mkrs(V, 1376 * 2752), rs = mkrs(sc, 4 * 2752), rz = mkrs(zr, 4 * 2752);
        const int kg = l >> 3, cq = l & 7;
        v2u vr[3][8];
        v4u sr[3], zreg[3];
        int un[3];
#pragma unroll
        for (int rd = 0; rd < 3; ++rd) {
          int i = rd * 8 + w + g * 5 + (P & 1) * 3;
          if (i >= 22) i -= 22;
          un[rd] = i;
          if (rd < 2 || w < 6) {
            const int k8 = i * 8 + kg;
            const bool ok = k8 < 172;
            const int vo = ok ? k8 * (8 * 2752) + P * 64 + cq * 8 : OOB;
            const int so = ok ? g * 2752 + k8 * 16 : OOB;
            sr[rd] = __builtin_amdgcn_raw_buffer_load_b128(rs, so, 0, 0);
            zreg[rd] = __builtin_amdgcn_raw_buffer_load_b128(rz, so, 0, 0);
#pragma unroll
            for (int e = 0; e < 8; ++e) vr[rd][e] = __builtin_amdgcn_raw_buffer_load_b64(rv, vo, e * 2752, 0);
          }
        }
        if (w != 7) {
          asm volatile("" ::"s"(tlo), "s"(thi));
          bar_lgkm();
        }
        __builtin_amdgcn_sched_barrier(0);
        const int uu = (cq & 4) | ((cq & 1) << 1) | ((cq >> 1) & 1);
        v4f ca[4] = {};
#pragma unroll
        for (int rd = 0; rd < 3; ++rd) {
          if (rd < 2 || w < 6) {
            float s[8];
#pragma unroll
            for (int t = 0; t < 4; ++t) { s[2 * t] = flo(sr[rd][t]); s[2 * t + 1] = fhi(sr[rd][t]); }
            v4u za;
#pragma unroll
            for (int t = 0; t < 4; ++t) za[t] = (l & 1) ? 0x43004300u : zreg[rd][t];
            LAS char* dst = smem + (un[rd] * 8 + kg) * 512;
#pragma unroll
            for (int c = 0; c < 4; ++c) {
              v4u o;
#pragma unroll
              for (int t = 0; t < 4; ++t) {
                u32 a0 = vr[rd][2 * t][c >> 1], a1 = vr[rd][2 * t + 1][c >> 1];
                float f0 = (c & 1) ? fhi(a0) : flo(a0), f1 = (c & 1) ? fhi(a1) : flo(a1);
                o[t] = cvtpk(f0 * s[2 * t], f1 * s[2 * t + 1]);
              }
              *(LAS v4u*)(dst + (8 * c + (uu ^ (c >> 1))) * 16) = o;
              ca[c] = __builtin_amdgcn_mfma_f32_4x4x4bf16_1k(__builtin_bit_cast(v4s, (v2u){za[0], za[1]}), __builtin_bit_cast(v4s, (v2u){o[0], o[1]}), ca[c], 0, 0, 0);
              ca[c] = __builtin_amdgcn_mfma_f32_4x4x4bf16_1k(__builtin_bit_cast(v4s, (v2u){za[2], za[3]}), __builtin_bit_cast(v4s, (v2u){o[2], o[3]}), ca[c], 0, 0, 0);
            }
          }
        }
        {
          u32 cv[4];
#pragma unroll
          for (int c = 0; c < 4; ++c) cv[c] = __builtin_bit_cast(u32, ca[c][0] + ca[c][1]);
          auto sa = __builtin_amdgcn_permlane16_swap(cv[0], cv[1], false, false);
          auto sb = __builtin_amdgcn_permlane16_swap(cv[2], cv[3], false, false);
          const float fa_ = __builtin_bit_cast(float, (u32)sa[0]) + __builtin_bit_cast(float, (u32)sa[1]);
          const float fb_ = __builtin_bit_cast(float, (u32)sb[0]) + __builtin_bit_cast(float, (u32)sb[1]);
          auto sc_ = __builtin_amdgcn_permlane32_swap(__builtin_bit_cast(u32, fa_), __builtin_bit_cast(u32, fb_), false, false);
          float v = __builtin_bit_cast(float, (u32)sc_[0]) + __builtin_bit_cast(float, (u32)sc_[1]);
          v += __builtin_bit_cast(float, __builtin_amdgcn_mov_dpp(__builtin_bit_cast(u32, v), 0x128, 0xf, 0xf, true));
          *(LAS float*)(smem + L_CB + w * 128 + (l & 7) * 16 + (l >> 4) * 4) = v;
        }
        ST(1);
        const int mi = w & 3, kh = w >> 2;
        const int hh = (g * 4 + mi) * 2 + kh;
        poll(rw, l < 11 ? OFF_QF + hh * 128 + l * 8 : OOB, l < 11, tlo, thi);
        ST(2);
        v4u qr[11];
#pragma unroll
        for (int j = 0; j < 11; ++j) qr[j] = __builtin_amdgcn_raw_buffer_load_b128(rw, OFF_QP + hh * 11264 + l * 16, j * 1024, SC1);
        bar_lgkm();
        const int sl = l & 31;
        v16f acc = {};
        if (kh) {
          const int u2 = sl >> 2;
          const int col = 4 * ((u2 & 4) | ((u2 & 1) << 1) | ((u2 >> 1) & 1)) + (sl & 3);
          float c = 0.f;
#pragma unroll
          for (int j = 0; j < 8; ++j) c -= *(LAS float*)(smem + L_CB + j * 128 + col * 4);
#pragma unroll
          for (int j = 0; j < 16; ++j) acc[j] = c;
        }
        LAS char* bsrc = smem + kh * 45056 + (l >> 5) * 512 + (8 * (sl & 3) + ((sl >> 2) ^ ((sl & 3) >> 1))) * 16;
        auto step = [&](int s, v4u bfr) {
          const u32 qv = qr[s >> 2][s & 3];
          v4u a;
#pragma unroll
          for (int t = 0; t < 4; ++t) a[t] = ((qv >> (4 * t)) & 0x000F000Fu) | 0x43004300u;
          acc = mfma32(a, bfr, acc);
        };
        v4u bf[4];
#pragma unroll
        for (int s = 0; s < 3; ++s) bf[s] = lds128(bsrc + s * 1024);
#pragma unroll
        for (int s = 0; s < 42; ++s) {
          if (s + 3 < 44) bf[(s + 3) & 3] = lds128(bsrc + (s + 3) * 1024);
          step(s, bf[s & 3]);
          __builtin_amdgcn_sched_barrier(0);
        }
        if (!kh) {
          step(42, bf[42 & 3]);
          step(43, bf[43 & 3]);
#pragma unroll
          for (int j = 0; j < 4; ++j)
            *(LAS v4f*)(smem + L_RED + mi * 4096 + j * 1024 + l * 16) = (v4f){acc[4 * j], acc[4 * j + 1], acc[4 * j + 2], acc[4 * j + 3]};
        }
        bar_lgkm();
        ST(3);
        if (kh) {
          v4f o4[4];
#pragma unroll
          for (int j = 0; j < 4; ++j) o4[j] = *(LAS v4f*)(smem + L_RED + mi * 4096 + j * 1024 + l * 16);
#pragma unroll
          for (int i = 0; i < 2; ++i) {
            v4u o;
#pragma unroll
            for (int t = 0; t < 4; ++t) {
              const int j = 8 * i + 2 * t;
              o[t] = cvtpk(acc[j] + o4[j >> 2][j & 3], acc[j + 1] + o4[j >> 2][(j & 3) + 1]);
            }
            __builtin_amdgcn_raw_buffer_store_b128(o, rw, OFF_T + P * 32768 + (g * 8 + mi * 2 + i) * 1024 + l * 16, 0, SC1);
          }
          drain();
          if (l == 0) {
            u32 c = __hip_atomic_fetch_add((LAS u32*)(smem + L_CNT), 1u, __ATOMIC_RELAXED, __HIP_MEMORY_SCOPE_WORKGROUP);
            if (c == 3) __builtin_amdgcn_raw_buffer_store_b64(tagv, rw, OFF_TF + g * 512 + P * 8, 0, SC1);
          }
        }
      } else if (w != 7) {
        asm volatile("" ::"s"(tlo), "s"(thi));
        bar_lgkm();
      }
    }
  }
  if (PH & 4) {
    const int rp = (xcd >> 1) * 4 + (r & 3), ct = (xcd & 1) * 8 + (r >> 2);
    if (ct < 15) {
      if (w >= 4) {
        const int g = w - 4;
        ST(4);
        poll(rw, l < 3 ? OFF_TF + g * 512 + min(3 * ct + l, 42) * 8 : OOB, l < 3, tlo, thi);
        ST(5);
        v4u t[8][3];
        const int tb = OFF_T + g * 8192 + l * 16;
        const int p0 = 3 * ct * 32768, p1 = min(3 * ct + 1, 42) * 32768, p2 = min(3 * ct + 2, 42) * 32768;
#pragma unroll
        for (int c = 0; c < 8; ++c) {
          t[c][0] = __builtin_amdgcn_raw_buffer_load_b128(rw, tb + (c & 3) * 1024 + (c >> 2) * 4096, p0, SC1);
          t[c][1] = __builtin_amdgcn_raw_buffer_load_b128(rw, tb + (c & 3) * 1024 + (c >> 2) * 4096, p1, SC1);
          t[c][2] = __builtin_amdgcn_raw_buffer_load_b128(rw, tb + (c & 3) * 1024 + (c >> 2) * 4096, p2, SC1);
        }
        __builtin_amdgcn_sched_barrier(0);
#pragma unroll
        for (int c = 0; c < 8; ++c) {
#pragma unroll
          for (int pj = 0; pj < 3; ++pj) *(LAS v4u*)(smem + ((c * 4 + g) * 3 + pj) * 1024 + l * 16) = t[c][pj];
          bar_lgkm();
        }
        if (b == 0 && tid == 256) __builtin_amdgcn_raw_buffer_store_b64(tagv, rw, 0, 0, SC1);
        ST(6);
      } else {
        ST(4);
        poll(rw, l < 4 ? OFF_XF + rp * 128 + l * 8 : OOB, l < 4, tlo, thi);
        ST(5);
        v4u X[32];
        const int xb = OFF_XR + (rp * 4 + w) * 32768 + l * 16;
#pragma unroll
        for (int c = 0; c < 8; ++c)
#pragma unroll
          for (int g = 0; g < 4; ++g)
            X[c * 4 + g] = __builtin_amdgcn_raw_buffer_load_b128(rw, xb + (c & 3) * 1024, g * 8192 + (c >> 2) * 4096, SC1);
        v16f acc[3] = {};
        v4u fa[3][3];
        LAS char* ab = smem + l * 16;
        bar();
#pragma unroll
        for (int n = 0; n < 2; ++n)
#pragma unroll
          for (int pj = 0; pj < 3; ++pj) fa[n][pj] = lds128(ab + (n * 3 + pj) * 1024);
#pragma unroll
        for (int n = 0; n < 32; ++n) {
          if (n + 2 < 32) {
            if (((n + 2) & 3) == 0) bar();
#pragma unroll
            for (int pj = 0; pj < 3; ++pj) fa[(n + 2) % 3][pj] = lds128(ab + ((n + 2) * 3 + pj) * 1024);
          }
          __builtin_amdgcn_sched_barrier(0);
#pragma unroll
          for (int pj = 0; pj < 3; ++pj) acc[pj] = mfma32(fa[n % 3][pj], X[n], acc[pj]);
          __builtin_amdgcn_sched_barrier(0);
        }
        ST(6);
        LAS char* eb = smem + L_EP + w * (32 * 208);
#pragma unroll
        for (int pj = 0; pj < 3; ++pj)
#pragma unroll
          for (int i = 0; i < 2; ++i) {
            v4u o;
#pragma unroll
            for (int t = 0; t < 4; ++t) o[t] = cvtpk(acc[pj][8 * i + 2 * t], acc[pj][8 * i + 2 * t + 1]);
            *(LAS v4u*)(eb + (l & 31) * 208 + (pj * 4 + 2 * i + (l >> 5)) * 16) = o;
          }
        asm volatile("s_waitcnt lgkmcnt(0)" ::: "memory");
        rsrc_t ry = mkrs(y, 2048 * 2752);
        const int yb = (rp * 128 + w * 32) * 2752 + ct * 192;
#pragma unroll
        for (int t = 0; t < 6; ++t) {
          const int idx = t * 64 + l, row = idx / 12, ch = idx - row * 12;
          v4u o = lds128(eb + row * 208 + ch * 16);
          const int vo = (ct == 14 && ch >= 4) ? OOB : row * 2752 + ch * 16;
          __builtin_amdgcn_raw_buffer_store_b128(o, ry, vo, yb, 0);
        }
      }
    }
  }
}

extern "C" void kernel_launch(void* const* d_in, const int* in_sizes, int n_in, void* d_out, int out_size, void* d_ws,
                              size_t ws_size, hipStream_t stream) {
  const u16* x = (const u16*)d_in[0];
  const int* qw = (const int*)d_in[1];
  const u16* sc = (const u16*)d_in[2];
  const u16* zr = (const u16*)d_in[3];
  const u16* U = (const u16*)d_in[4];
  const u16* V = (const u16*)d_in[5];
  (void)hipFuncSetAttribute((const void*)kmain<7>, hipFuncAttributeMaxDynamicSharedMemorySize, LDS_BYTES);
  kmain<7><<<256, 512, LDS_BYTES, stream>>>(x, qw, sc, zr, U, V, (u16*)d_out, (char*)d_ws);
}
```

```cpp
#include <hip/hip_runtime.h>
#include <stdint.h>

typedef unsigned u32;
typedef unsigned long long u64;
typedef unsigned short u16;
typedef u32 v4u __attribute__((ext_vector_type(4)));
typedef u32 v2u __attribute__((ext_vector_type(2)));
typedef float v4f __attribute__((ext_vector_type(4)));
typedef float v16f __attribute__((ext_vector_type(16)));
typedef __bf16 v8bf __attribute__((ext_vector_type(8)));
typedef __bf16 v2bf __attribute__((ext_vector_type(2)));
typedef float v2f __attribute__((ext_vector_type(2)));
typedef short v4s __attribute__((ext_vector_type(4)));
#define LAS __attribute__((address_space(3)))
#define CAS __attribute__((address_space(4)))
typedef __amdgpu_buffer_rsrc_t rsrc_t;

#define PHASES 7

constexpr int OFF_QF = 4096, OFF_XF = 8192, OFF_TF = 12288;
constexpr int OFF_QP = 65536, OFF_XR = 1 << 20, OFF_T = 4 << 20, WS_BYTES = 6 << 20;
constexpr int SC1 = 16;
constexpr int OOB = 0x40000000;
constexpr int L_RED = 98304, L_CB = 114688, L_EP = 98304, L_CNT = 131072, LDS_BYTES = 131072 + 64;
constexpr u32 SPIN_MAX = 1u << 16;

__device__ __forceinline__ rsrc_t mkrs(const void* p, int bytes) {
  return __builtin_amdgcn_make_buffer_rsrc((void*)p, (short)0, bytes, 0x00020000);
}
__device__ __forceinline__ v16f mfma32(v4u a, v4u b, v16f c) {
  return __builtin_amdgcn_mfma_f32_32x32x16_bf16(__builtin_bit_cast(v8bf, a), __builtin_bit_cast(v8bf, b), c, 0, 0, 0);
}
__device__ __forceinline__ u32 cvtpk(float a, float b) {
  v2f f = {a, b};
  return __builtin_bit_cast(u32, __builtin_convertvector(f, v2bf));
}
__device__ __forceinline__ float flo(u32 v) { return __builtin_bit_cast(float, v << 16); }
__device__ __forceinline__ float fhi(u32 v) { return __builtin_bit_cast(float, v & 0xffff0000u); }
__device__ __forceinline__ void bar() { asm volatile("s_barrier" ::: "memory"); }
__device__ __forceinline__ void bar_lgkm() { asm volatile("s_waitcnt lgkmcnt(0)\n\ts_barrier" ::: "memory"); }
__device__ __forceinline__ void drain() { asm volatile("s_waitcnt vmcnt(0)" ::: "memory"); }
template <int N> __device__ __forceinline__ void wvm() { asm volatile("s_waitcnt vmcnt(%0)" ::"n"(N) : "memory"); }
__device__ __forceinline__ v4u lds128(LAS char* p) { return *(LAS v4u*)p; }

__device__ __forceinline__ void poll(rsrc_t rw, int off, bool act, u32 tlo, u32 thi) {
  for (u32 it = 0; it < SPIN_MAX; ++it) {
    v2u f = __builtin_amdgcn_raw_buffer_load_b64(rw, off, 0, SC1);
    bool ok = !act || (f.x == tlo && f.y == thi);
    if (__builtin_amdgcn_ballot_w64(!ok) == 0) break;
    asm volatile("" ::: "memory");
  }
  __builtin_amdgcn_fence(__ATOMIC_ACQUIRE, "wavefront");
  asm volatile("" ::: "memory");
}

#define ST(i)

__device__ __forceinline__ u32 qxor1(u32 v) { return __builtin_amdgcn_mov_dpp(v, 0xB1, 0xf, 0xf, true); }
__device__ __forceinline__ u32 qxor2(u32 v) { return __builtin_amdgcn_mov_dpp(v, 0x4E, 0xf, 0xf, true); }

__device__ __forceinline__ void pack_unit(const int* qw, rsrc_t rw, int u, int l) {
  rsrc_t rq = mkrs(qw, 512 * 5504);
  int rt = u / 22, un = u - rt * 22;
  int voff = (rt * 32 + (l & 28)) * 5504 + un * 256 + (l >> 5) * 128 + (l & 3) * 16;
  if (un == 21 && l >= 32) voff = OOB;
  v4u a[4], b[4];
#pragma unroll
  for (int r = 0; r < 4; ++r) {
    a[r] = __builtin_amdgcn_raw_buffer_load_b128(rq, voff, r * 5504, 0);
    b[r] = __builtin_amdgcn_raw_buffer_load_b128(rq, voff + 64, r * 5504, 0);
  }
  u32 D[4];
#pragma unroll
  for (int r = 0; r < 4; ++r)
    D[r] = a[r].x | (a[r].z << 4) | (b[r].x << 8) | (b[r].z << 12) | (a[r].y << 16) | (a[r].w << 20) | (b[r].y << 24) | (b[r].w << 28);
  bool o1 = l & 1, o2 = l & 2;
  u32 x0 = qxor1(D[0]), x1 = qxor1(D[1]), x2 = qxor1(D[2]), x3 = qxor1(D[3]);
  u32 E0 = o1 ? x1 : D[0], E1 = o1 ? D[1] : x0;
  u32 E2 = o1 ? x3 : D[2], E3 = o1 ? D[3] : x2;
  u32 y0 = qxor2(E0), y1 = qxor2(E1), y2 = qxor2(E2), y3 = qxor2(E3);
  u32 G0 = o2 ? y2 : E0, G2 = o2 ? E2 : y0;
  u32 G1 = o2 ? y3 : E1, G3 = o2 ? E3 : y1;
  u32 p0 = __builtin_amdgcn_perm(G1, G0, 0x06020400), p2 = __builtin_amdgcn_perm(G1, G0, 0x07030501);
  u32 p1 = __builtin_amdgcn_perm(G3, G2, 0x06020400), p3 = __builtin_amdgcn_perm(G3, G2, 0x07030501);
  auto s02 = __builtin_amdgcn_permlane32_swap(p0, p1, false, false);
  auto s13 = __builtin_amdgcn_permlane32_swap(p2, p3, false, false);
  v4u o = {s02[0], s13[0], s02[1], s13[1]};
  __builtin_amdgcn_raw_buffer_store_b128(o, rw, OFF_QP + u * 1024 + l * 16, 0, SC1);
}
__device__ __forceinline__ int qflag_off(int u) {
  int hh = u / 11;
  return OFF_QF + hh * 128 + (u - hh * 11) * 8;
}

template <int PH>
__global__ __launch_bounds__(512, 2) void kmain(const u16* __restrict__ x, const int* __restrict__ qw,
                                                const u16* __restrict__ sc, const u16* __restrict__ zr,
                                                const u16* __restrict__ U, const u16* __restrict__ V,
                                                u16* __restrict__ y, char* ws) {
  extern __shared__ __attribute__((aligned(16))) char smem_[];
  LAS char* smem = (LAS char*)smem_;
  const u64 t00 = __builtin_amdgcn_s_memrealtime();
  const int b = blockIdx.x, tid = threadIdx.x;
  const int w = __builtin_amdgcn_readfirstlane(tid >> 6), l = tid & 63;
  const int xcd = b & 7, r = b >> 3;
  const u64 tag = *(const CAS u64*)ws + 1;
  const u32 tlo = (u32)tag, thi = (u32)(tag >> 32);
  const v2u tagv = {tlo, thi};
  rsrc_t rw = mkrs(ws, WS_BYTES);
  if (tid == 0) *(LAS u32*)(smem + L_CNT) = 0;

  if (xcd < 2) {
    const int rp = xcd * 8 + (r >> 2), ep = r & 3;
    if (w >= 4) {
      const int lw = w - 4;
      rsrc_t rx = mkrs(x, 2048 * 1024), ru = mkrs(U, 512 * 1024);
      const int vx = (rp * 128 + lw * 32 + (l >> 3)) * 1024 + (((l & 7) ^ (l >> 4)) * 16);
      const int vu = (lw * 16 + (l >> 4)) * 1024 + ep * 256 + (((l & 15) ^ ((l >> 4) << 2)) * 16);
      auto issue = [&](int s) {
        LAS char* dst = smem + (s & 3) * 32768 + lw * 4096;
#pragma unroll
        for (int a = 0; a < 4; ++a) {
          __builtin_amdgcn_raw_ptr_buffer_load_lds(ru, (LAS void*)(dst + 16384 + a * 1024), 16, (vu ^ (a * 16)) + a * 4096, s * 65536, 0, 0);
          __builtin_amdgcn_raw_ptr_buffer_load_lds(rx, (LAS void*)(dst + a * 1024), 16, (vx ^ ((a & 1) * 64)) + a * 8192, s * 128, 0, 0);
        }
      };
      if (PH & 2) { issue(0); issue(1); issue(2); issue(3); }
      asm volatile("" ::"s"(tlo), "s"(thi));
      bar_lgkm();
      if (PH & 2) {
        wvm<24>(); bar();
        wvm<16>(); bar(); issue(4);
        wvm<16>(); bar(); issue(5);
        wvm<16>(); bar(); issue(6);
        wvm<16>(); bar(); issue(7);
        wvm<16>(); bar();
        wvm<8>(); bar();
        wvm<0>(); bar();
        bar();
      }
    } else {
      if ((PH & 1) && w == 3) { __builtin_amdgcn_s_setprio(3); pack_unit(qw, rw, 172 + xcd * 32 + r, l); }
      asm volatile("" ::"s"(tlo), "s"(thi));
      bar_lgkm();
      if ((PH & 1) && w == 3) {
        drain();
        if (l == 0) __builtin_amdgcn_raw_buffer_store_b64(tagv, rw, qflag_off(172 + xcd * 32 + r), 0, SC1);
        __builtin_amdgcn_s_setprio(0);
      }
      if (PH & 2) {
        const int we = w & 1, wm = w >> 1, h = l >> 5;
        const int rowb = wm * 64 + (l & 31);
        const int xb = rowb * 128 + ((h ^ ((rowb >> 1) & 7)) * 16);
        const int q = (l & 15) >> 2, p = l & 3, blk = (l >> 4) & 1;
        const int ub = 16384 + 256 * (8 * h + q) + 16 * ((((2 * we) ^ q) << 2) | ((2 * blk + (p >> 1)) ^ (2 * h))) + 8 * (p & 1);
        v16f acc[2][2] = {};
        v4u fa[2][2], fb[2][2];
        auto rd = [&](int n, int bi) {
          const int so = ((n >> 2) & 3) * 32768, ks = n & 3;
#pragma unroll
          for (int ce = 0; ce < 2; ++ce) {
            v2u t0 = __builtin_bit_cast(v2u, __builtin_amdgcn_ds_read_tr16_b64_v4i16((LAS v4s*)(smem + ((ub ^ (ce * 64)) + so + ks * 4096))));
            v2u t1 = __builtin_bit_cast(v2u, __builtin_amdgcn_ds_read_tr16_b64_v4i16((LAS v4s*)(smem + ((ub ^ (ce * 64) ^ 16) + so + ks * 4096 + 1024))));
            fa[bi][ce] = (v4u){t0.x, t0.y, t1.x, t1.y};
            fb[bi][ce] = lds128(smem + ((xb ^ (ks * 32)) + so + ce * 4096));
          }
        };
        bar();
        rd(0, 0);
#pragma unroll
        for (int n = 0; n < 32; ++n) {
          if (n + 1 < 32) {
            if (((n + 1) & 3) == 0) bar_lgkm();
            rd(n + 1, (n + 1) & 1);
          }
          __builtin_amdgcn_sched_barrier(0);
#pragma unroll
          for (int ce = 0; ce < 2; ++ce)
#pragma unroll
            for (int cm = 0; cm < 2; ++cm) acc[ce][cm] = mfma32(fa[n & 1][ce], fb[n & 1][cm], acc[ce][cm]);
          __builtin_amdgcn_sched_barrier(0);
        }
        bar();
        ST(1);
        const int ob = OFF_XR + (rp * 4 + wm * 2) * 32768 + (ep * 8 + we * 4) * 1024 + l * 16;
#pragma unroll
        for (int cm = 0; cm < 2; ++cm)
#pragma unroll
          for (int ce = 0; ce < 2; ++ce)
#pragma unroll
            for (int i = 0; i < 2; ++i) {
              v4u o;
#pragma unroll
              for (int t = 0; t < 4; ++t) o[t] = cvtpk(acc[ce][cm][8 * i + 2 * t], acc[ce][cm][8 * i + 2 * t + 1]);
              __builtin_amdgcn_raw_buffer_store_b128(o, rw, ob + (ce * 2 + i) * 1024, cm * 32768, SC1);
            }
        drain();
        if (l == 0) {
          u32 c = __hip_atomic_fetch_add((LAS u32*)(smem + L_CNT), 1u, __ATOMIC_RELAXED, __HIP_MEMORY_SCOPE_WORKGROUP);
          if (c == 3) __builtin_amdgcn_raw_buffer_store_b64(tagv, rw, OFF_XF + rp * 128 + ep * 8, 0, SC1);
        }
        ST(2);
      }
    }
  } else {
    const int xi = xcd - 2;
    const int rr = xi < 5 ? r - 4 : r;
    if (rr < 0) {
      const int u0 = 236 + (xi * 4 + r) * 6;
      if ((PH & 1) && w < 6 && u0 + w < 352) pack_unit(qw, rw, u0 + w, l);
      asm volatile("" ::"s"(tlo), "s"(thi));
      drain();
      bar_lgkm();
      if ((PH & 1) && w == 0 && l < 6 && u0 + l < 352) __builtin_amdgcn_raw_buffer_store_b64(tagv, rw, qflag_off(u0 + l), 0, SC1);
    } else {
      const int P = xi * 7 + (rr >> 2), g = rr & 3;
      if (w == 7) {
        if (PH & 1) { __builtin_amdgcn_s_setprio(3); pack_unit(qw, rw, P * 4 + g, l); }
        asm volatile("" ::"s"(tlo), "s"(thi));
        bar_lgkm();
        if (PH & 1) {
          drain();
          if (l == 0) __builtin_amdgcn_raw_buffer_store_b64(tagv, rw, qflag_off(P * 4 + g), 0, SC1);
          __builtin_amdgcn_s_setprio(0);
        }
      }
      if (PH & 2) {
        rsrc_t rv = mkrs(V, 1376 * 2752), rs = mkrs(sc, 4 * 2752), rz = mkrs(zr, 4 * 2752);
        const int kg = l >> 3, cq = l & 7;
        v2u vr[3][8];
        v4u sr[3], zreg[3];
        int un[3];
#pragma unroll
        for (int rd = 0; rd < 3; ++rd) {
          int i = rd * 8 + w + g * 5 + (P & 1) * 3;
          if (i >= 22) i -= 22;
          un[rd] = i;
          if (rd < 2 || w < 6) {
            const int k8 = i * 8 + kg;
            const bool ok = k8 < 172;
            const int vo = ok ? k8 * (8 * 2752) + P * 64 + cq * 8 : OOB;
            const int so = ok ? g * 2752 + k8 * 16 : OOB;
            sr[rd] = __builtin_amdgcn_raw_buffer_load_b128(rs, so, 0, 0);
            zreg[rd] = __builtin_amdgcn_raw_buffer_load_b128(rz, so, 0, 0);
#pragma unroll
            for (int e = 0; e < 8; ++e) vr[rd][e] = __builtin_amdgcn_raw_buffer_load_b64(rv, vo, e * 2752, 0);
          }
        }
        if (w != 7) {
          asm volatile("" ::"s"(tlo), "s"(thi));
          bar_lgkm();
        }
        __builtin_amdgcn_sched_barrier(0);
        const int uu = (cq & 4) | ((cq & 1) << 1) | ((cq >> 1) & 1);
        v4f ca[4] = {};
#pragma unroll
        for (int rd = 0; rd < 3; ++rd) {
          if (rd < 2 || w < 6) {
            float s[8];
#pragma unroll
            for (int t = 0; t < 4; ++t) { s[2 * t] = flo(sr[rd][t]); s[2 * t + 1] = fhi(sr[rd][t]); }
            v4u za;
#pragma unroll
            for (int t = 0; t < 4; ++t) za[t] = (l & 1) ? 0x43004300u : zreg[rd][t];
            LAS char* dst = smem + (un[rd] * 8 + kg) * 512;
#pragma unroll
            for (int c = 0; c < 4; ++c) {
              v4u o;
#pragma unroll
              for (int t = 0; t < 4; ++t) {
                u32 a0 = vr[rd][2 * t][c >> 1], a1 = vr[rd][2 * t + 1][c >> 1];
                float f0 = (c & 1) ? fhi(a0) : flo(a0), f1 = (c & 1) ? fhi(a1) : flo(a1);
                o[t] = cvtpk(f0 * s[2 * t], f1 * s[2 * t + 1]);
              }
              *(LAS v4u*)(dst + (8 * c + (uu ^ (c >> 1))) * 16) = o;
              ca[c] = __builtin_amdgcn_mfma_f32_4x4x4bf16_1k(__builtin_bit_cast(v4s, (v2u){za[0], za[1]}), __builtin_bit_cast(v4s, (v2u){o[0], o[1]}), ca[c], 0, 0, 0);
              ca[c] = __builtin_amdgcn_mfma_f32_4x4x4bf16_1k(__builtin_bit_cast(v4s, (v2u){za[2], za[3]}), __builtin_bit_cast(v4s, (v2u){o[2], o[3]}), ca[c], 0, 0, 0);
            }
          }
        }
        {
          u32 cv[4];
#pragma unroll
          for (int c = 0; c < 4; ++c) cv[c] = __builtin_bit_cast(u32, ca[c][0] + ca[c][1]);
          auto sa = __builtin_amdgcn_permlane16_swap(cv[0], cv[1], false, false);
          auto sb = __builtin_amdgcn_permlane16_swap(cv[2], cv[3], false, false);
          const float fa_ = __builtin_bit_cast(float, (u32)sa[0]) + __builtin_bit_cast(float, (u32)sa[1]);
          const float fb_ = __builtin_bit_cast(float, (u32)sb[0]) + __builtin_bit_cast(float, (u32)sb[1]);
          auto sc_ = __builtin_amdgcn_permlane32_swap(__builtin_bit_cast(u32, fa_), __builtin_bit_cast(u32, fb_), false, false);
          float v = __builtin_bit_cast(float, (u32)sc_[0]) + __builtin_bit_cast(float, (u32)sc_[1]);
          v += __builtin_bit_cast(float, __builtin_amdgcn_mov_dpp(__builtin_bit_cast(u32, v), 0x128, 0xf, 0xf, true));
          *(LAS float*)(smem + L_CB + w * 128 + (l & 7) * 16 + (l >> 4) * 4) = v;
        }
        ST(1);
        const int mi = w & 3, kh = w >> 2;
        const int hh = (g * 4 + mi) * 2 + kh;
        poll(rw, l < 11 ? OFF_QF + hh * 128 + l * 8 : OOB, l < 11, tlo, thi);
        ST(2);
        v4u qr[11];
#pragma unroll
        for (int j = 0; j < 11; ++j) qr[j] = __builtin_amdgcn_raw_buffer_load_b128(rw, OFF_QP + hh * 11264 + l * 16, j * 1024, SC1);
        bar_lgkm();
        const int sl = l & 31;
        v16f acc = {};
        if (kh) {
          const int u2 = sl >> 2;
          const int col = 4 * ((u2 & 4) | ((u2 & 1) << 1) | ((u2 >> 1) & 1)) + (sl & 3);
          float c = 0.f;
#pragma unroll
          for (int j = 0; j < 8; ++j) c -= *(LAS float*)(smem + L_CB + j * 128 + col * 4);
#pragma unroll
          for (int j = 0; j < 16; ++j) acc[j] = c;
        }
        LAS char* bsrc = smem + kh * 45056 + (l >> 5) * 512 + (8 * (sl & 3) + ((sl >> 2) ^ ((sl & 3) >> 1))) * 16;
        auto step = [&](int s, v4u bfr) {
          const u32 qv = qr[s >> 2][s & 3];
          v4u a;
#pragma unroll
          for (int t = 0; t < 4; ++t) a[t] = ((qv >> (4 * t)) & 0x000F000Fu) | 0x43004300u;
          acc = mfma32(a, bfr, acc);
        };
        v4u bf[4];
#pragma unroll
        for (int s = 0; s < 3; ++s) bf[s] = lds128(bsrc + s * 1024);
#pragma unroll
        for (int s = 0; s < 42; ++s) {
          if (s + 3 < 44) bf[(s + 3) & 3] = lds128(bsrc + (s + 3) * 1024);
          step(s, bf[s & 3]);
          __builtin_amdgcn_sched_barrier(0);
        }
        if (!kh) {
          step(42, bf[42 & 3]);
          step(43, bf[43 & 3]);
#pragma unroll
          for (int j = 0; j < 4; ++j)
            *(LAS v4f*)(smem + L_RED + mi * 4096 + j * 1024 + l * 16) = (v4f){acc[4 * j], acc[4 * j + 1], acc[4 * j + 2], acc[4 * j + 3]};
        }
        bar_lgkm();
        ST(3);
        if (kh) {
          v4f o4[4];
#pragma unroll
          for (int j = 0; j < 4; ++j) o4[j] = *(LAS v4f*)(smem + L_RED + mi * 4096 + j * 1024 + l * 16);
#pragma unroll
          for (int i = 0; i < 2; ++i) {
            v4u o;
#pragma unroll
            for (int t = 0; t < 4; ++t) {
              const int j = 8 * i + 2 * t;
              o[t] = cvtpk(acc[j] + o4[j >> 2][j & 3], acc[j + 1] + o4[j >> 2][(j & 3) + 1]);
            }
            __builtin_amdgcn_raw_buffer_store_b128(o, rw, OFF_T + P * 32768 + (g * 8 + mi * 2 + i) * 1024 + l * 16, 0, SC1);
          }
          drain();
          if (l == 0) {
            u32 c = __hip_atomic_fetch_add((LAS u32*)(smem + L_CNT), 1u, __ATOMIC_RELAXED, __HIP_MEMORY_SCOPE_WORKGROUP);
            if (c == 3) __builtin_amdgcn_raw_buffer_store_b64(tagv, rw, OFF_TF + g * 512 + P * 8, 0, SC1);
          }
        }
      } else if (w != 7) {
        asm volatile("" ::"s"(tlo), "s"(thi));
        bar_lgkm();
      }
    }
  }
  if (PH & 4) {
    const int rp = (xcd >> 1) * 4 + (r & 3), ct = (xcd & 1) * 8 + (r >> 2);
    if (ct < 15) {
      if (w >= 4) {
        const int g = w - 4;
        ST(4);
        poll(rw, l < 3 ? OFF_TF + g * 512 + min(3 * ct + l, 42) * 8 : OOB, l < 3, tlo, thi);
        ST(5);
        v4u t[8][3];
        const int tb = OFF_T + g * 8192 + l * 16;
        const int p0 = 3 * ct * 32768, p1 = min(3 * ct + 1, 42) * 32768, p2 = min(3 * ct + 2, 42) * 32768;
#pragma unroll
        for (int c = 0; c < 8; ++c) {
          t[c][0] = __builtin_amdgcn_raw_buffer_load_b128(rw, tb + (c & 3) * 1024 + (c >> 2) * 4096, p0, SC1);
          t[c][1] = __builtin_amdgcn_raw_buffer_load_b128(rw, tb + (c & 3) * 1024 + (c >> 2) * 4096, p1, SC1);
          t[c][2] = __builtin_amdgcn_raw_buffer_load_b128(rw, tb + (c & 3) * 1024 + (c >> 2) * 4096, p2, SC1);
        }
        __builtin_amdgcn_sched_barrier(0);
#pragma unroll
        for (int c = 0; c < 8; ++c) {
#pragma unroll
          for (int pj = 0; pj < 3; ++pj) *(LAS v4u*)(smem + ((c * 4 + g) * 3 + pj) * 1024 + l * 16) = t[c][pj];
          bar_lgkm();
        }
        if (b == 0 && tid == 256) __builtin_amdgcn_raw_buffer_store_b64(tagv, rw, 0, 0, SC1);
        ST(6);
      } else {
        ST(4);
        poll(rw, l < 4 ? OFF_XF + rp * 128 + l * 8 : OOB, l < 4, tlo, thi);
        ST(5);
        v4u X[32];
        const int xb = OFF_XR + (rp * 4 + w) * 32768 + l * 16;
#pragma unroll
        for (int c = 0; c < 8; ++c)
#pragma unroll
          for (int g = 0; g < 4; ++g)
            X[c * 4 + g] = __builtin_amdgcn_raw_buffer_load_b128(rw, xb + (c & 3) * 1024, g * 8192 + (c >> 2) * 4096, SC1);
        v16f acc[3] = {};
        v4u fa[3][3];
        LAS char* ab = smem + l * 16;
        bar();
#pragma unroll
        for (int n = 0; n < 2; ++n)
#pragma unroll
          for (int pj = 0; pj < 3; ++pj) fa[n][pj] = lds128(ab + (n * 3 + pj) * 1024);
#pragma unroll
        for (int n = 0; n < 32; ++n) {
          if (n + 2 < 32) {
            if (((n + 2) & 3) == 0) bar();
#pragma unroll
            for (int pj = 0; pj < 3; ++pj) fa[(n + 2) % 3][pj] = lds128(ab + ((n + 2) * 3 + pj) * 1024);
          }
          __builtin_amdgcn_sched_barrier(0);
#pragma unroll
          for (int pj = 0; pj < 3; ++pj) acc[pj] = mfma32(fa[n % 3][pj], X[n], acc[pj]);
          __builtin_amdgcn_sched_barrier(0);
        }
        ST(6);
        LAS char* eb = smem + L_EP + w * (32 * 208);
#pragma unroll
        for (int pj = 0; pj < 3; ++pj)
#pragma unroll
          for (int i = 0; i < 2; ++i) {
            v4u o;
#pragma unroll
            for (int t = 0; t < 4; ++t) o[t] = cvtpk(acc[pj][8 * i + 2 * t], acc[pj][8 * i + 2 * t + 1]);
            *(LAS v4u*)(eb + (l & 31) * 208 + (pj * 4 + 2 * i + (l >> 5)) * 16) = o;
          }
        asm volatile("s_waitcnt lgkmcnt(0)" ::: "memory");
        rsrc_t ry = mkrs(y, 2048 * 2752);
        const int yb = (rp * 128 + w * 32) * 2752 + ct * 192;
#pragma unroll
        for (int t = 0; t < 6; ++t) {
          const int idx = t * 64 + l, row = idx / 12, ch = idx - row * 12;
          v4u o = lds128(eb + row * 208 + ch * 16);
          const int vo = (ct == 14 && ch >= 4) ? OOB : row * 2752 + ch * 16;
          __builtin_amdgcn_raw_buffer_store_b128(o, ry, vo, yb, 16);
        }
      }
    }
  }
  drain();
  if (b == 0 && w == 0) while (__builtin_amdgcn_s_memrealtime() - t00 < 3000) __builtin_amdgcn_s_sleep(1);
}

extern "C" void kernel_launch(void* const* d_in, const int* in_sizes, int n_in, void* d_out, int out_size, void* d_ws,
                              size_t ws_size, hipStream_t stream) {
  const u16* x = (const u16*)d_in[0];
  const int* qw = (const int*)d_in[1];
  const u16* sc = (const u16*)d_in[2];
  const u16* zr = (const u16*)d_in[3];
  const u16* U = (const u16*)d_in[4];
  const u16* V = (const u16*)d_in[5];
  (void)hipFuncSetAttribute((const void*)kmain<7>, hipFuncAttributeMaxDynamicSharedMemorySize, LDS_BYTES);
  kmain<7><<<256, 512, LDS_BYTES, stream>>>(x, qw, sc, zr, U, V, (u16*)d_out, (char*)d_ws);
}
```

```cpp
#include <hip/hip_runtime.h>
#include <stdint.h>

typedef unsigned u32;
typedef unsigned long long u64;
typedef unsigned short u16;
typedef u32 v4u __attribute__((ext_vector_type(4)));
typedef u32 v2u __attribute__((ext_vector_type(2)));
typedef float v4f __attribute__((ext_vector_type(4)));
typedef float v16f __attribute__((ext_vector_type(16)));
typedef __bf16 v8bf __attribute__((ext_vector_type(8)));
typedef __bf16 v2bf __attribute__((ext_vector_type(2)));
typedef float v2f __attribute__((ext_vector_type(2)));
typedef short v4s __attribute__((ext_vector_type(4)));
#define LAS __attribute__((address_space(3)))
#define CAS __attribute__((address_space(4)))
#define GAS __attribute__((address_space(1)))
typedef __amdgpu_buffer_rsrc_t rsrc_t;

#define YAUX 16
#define NX1 8
#define SPREAD 1
#define CODEPF 1

constexpr int OFF_QF = 6 << 20, OFF_CF = 7 << 20;
constexpr int OFF_QP = 65536, OFF_XR = 1 << 20, OFF_T = 4 << 20, WS_BYTES = 8 << 20;
constexpr int CODE_LINES = 144;
constexpr int SC1 = 16;
constexpr int OOB = 0x40000000;
constexpr int L_RED = 98304, L_CB = 114688, L_SZ = 116736, L_EP = 98304, L_CNT = 131072, L_TAG = 131080, LDS_BYTES = 131072 + 64;

__device__ __forceinline__ rsrc_t mkrs(const void* p, int bytes) {
  return __builtin_amdgcn_make_buffer_rsrc((void*)p, (short)0, bytes, 0x00020000);
}
__device__ __forceinline__ v16f mfma32(v4u a, v4u b, v16f c) {
  return __builtin_amdgcn_mfma_f32_32x32x16_bf16(__builtin_bit_cast(v8bf, a), __builtin_bit_cast(v8bf, b), c, 0, 0, 0);
}
__device__ __forceinline__ v4f mfma4(u32 a0, u32 a1, u32 b0, u32 b1, v4f c) {
  return __builtin_amdgcn_mfma_f32_4x4x4bf16_1k(__builtin_bit_cast(v4s, (v2u){a0, a1}), __builtin_bit_cast(v4s, (v2u){b0, b1}), c, 0, 0, 0);
}
__device__ __forceinline__ u32 cvtpk(float a, float b) {
  v2f f = {a, b};
  return __builtin_bit_cast(u32, __builtin_convertvector(f, v2bf));
}
__device__ __forceinline__ float asf(u32 v) { return __builtin_bit_cast(float, v); }
__device__ __forceinline__ u32 asu(float v) { return __builtin_bit_cast(u32, v); }
__device__ __forceinline__ float flo(u32 v) { return asf(v << 16); }
__device__ __forceinline__ float fhi(u32 v) { return asf(v & 0xffff0000u); }
__device__ __forceinline__ void bar() { asm volatile("s_barrier" ::: "memory"); }
__device__ __forceinline__ void bar_lgkm() { asm volatile("s_waitcnt lgkmcnt(0)\n\ts_barrier" ::: "memory"); }
__device__ __forceinline__ void drain() { asm volatile("s_waitcnt vmcnt(0)" ::: "memory"); }
template <int N> __device__ __forceinline__ void wvm() { asm volatile("s_waitcnt vmcnt(%0)" ::"n"(N) : "memory"); }
__device__ __forceinline__ v4u lds128(LAS char* p) { return *(LAS v4u*)p; }

__device__ __forceinline__ void poll(rsrc_t rw, int off, u64 mask, v2u tag) {
  v2u f;
  u64 sv;
  u32 cnt;
  asm volatile(
      "s_mov_b64 %1, exec\n\ts_mov_b64 exec, %5\n\ts_mov_b32 %2, 0x10000\n"
      "1:\n\tbuffer_load_dwordx2 %0, %3, %4, 0 offen sc1\n\ts_waitcnt vmcnt(0)\n\t"
      "v_cmp_ne_u64 vcc, %0, %6\n\ts_cbranch_vccz 2f\n\ts_sleep 1\n\ts_sub_u32 %2, %2, 1\n\ts_cbranch_scc0 1b\n"
      "2:\n\ts_mov_b64 exec, %1"
      : "=&v"(f), "=&s"(sv), "=&s"(cnt)
      : "v"(off), "s"(rw), "s"(mask), "v"(tag)
      : "vcc", "scc", "memory");
}
__device__ __forceinline__ void poll2(rsrc_t rw, int off, u64 mask, v2u tag) {
  v2u f, h;
  u64 sv, m2;
  u32 cnt;
  asm volatile(
      "s_mov_b64 %1, exec\n\ts_mov_b64 exec, %7\n\ts_mov_b32 %2, 0x10000\n"
      "1:\n\tbuffer_load_dwordx2 %0, %5, %6, 0 offen sc1\n\tbuffer_load_dwordx2 %3, %5, %6, 0 offen offset:128 sc1\n\ts_waitcnt vmcnt(0)\n\t"
      "v_cmp_ne_u64 vcc, %0, %8\n\tv_cmp_ne_u64 %4, %3, %8\n\ts_or_b64 %4, %4, vcc\n\ts_cbranch_scc0 2f\n\ts_sleep 1\n\ts_sub_u32 %2, %2, 1\n\ts_cbranch_scc0 1b\n"
      "2:\n\ts_mov_b64 exec, %1"
      : "=&v"(f), "=&s"(sv), "=&s"(cnt), "=&v"(h), "=&s"(m2)
      : "v"(off), "s"(rw), "s"(mask), "v"(tag)
      : "vcc", "scc", "memory");
}

#define ST(i)

__device__ __forceinline__ u32 qxor1(u32 v) { return __builtin_amdgcn_mov_dpp(v, 0xB1, 0xf, 0xf, true); }
__device__ __forceinline__ u32 qxor2(u32 v) { return __builtin_amdgcn_mov_dpp(v, 0x4E, 0xf, 0xf, true); }

__global__ __launch_bounds__(512, 2) void kmain(const u16* x, const int* qw, const u16* sc, const u16* zr, const u16* U,
                                                const u16* V, u16* y, char* ws) {
  extern __shared__ __attribute__((aligned(16))) char smem_[];
  LAS char* smem = (LAS char*)smem_;
  ST(0);
  u64 pc;
  asm volatile("s_getpc_b64 %0" : "=s"(pc));
  asm volatile("" : "+s"(x), "+s"(qw), "+s"(sc), "+s"(zr), "+s"(U), "+s"(V), "+s"(y), "+s"(ws));
  const int b = blockIdx.x, tid = threadIdx.x;
  const int w = __builtin_amdgcn_readfirstlane(tid >> 6), l = tid & 63;
  const int xcd = b & 7, r = b >> 3;
  rsrc_t rw = mkrs(ws, WS_BYTES);
  int role, ra, rb, u;
  {
    const int j = xcd < 3 ? r : r - 4;
    role = j < 0 ? 2 : j < 8 ? 0 : 1;
    rb = j & 3;
    ra = role == 0 ? 2 * xcd + (j >> 2) : (xcd < 3 ? 6 * xcd : 3 + 5 * xcd) + (j >> 2) - 2;
    u = role == 2 ? 236 + ((xcd - 3) * 4 + r) * 6 + w : role == 0 ? 172 + ra * 4 + rb : ra * 4 + rb;
  }
  v2u tag;
  if (role == 2 ? (w < 6 && u < 352) : w == 0) {
    __builtin_amdgcn_s_setprio(3);
    const u64 E = *(const CAS u64*)ws;
    rsrc_t rq = mkrs(qw, 512 * 5504);
    const int rt = u / 22, un = u - rt * 22;
    int voff = (rt * 32 + (l & 28)) * 5504 + un * 256 + (l >> 5) * 128 + (l & 3) * 16;
    if (un == 21 && l >= 32) voff = OOB;
    v4u a[4], c[4];
#pragma unroll
    for (int i = 0; i < 4; ++i) {
      a[i] = __builtin_amdgcn_raw_buffer_load_b128(rq, voff, i * 5504, 0);
      c[i] = __builtin_amdgcn_raw_buffer_load_b128(rq, voff + 64, i * 5504, 0);
    }
    __builtin_amdgcn_sched_barrier(0);
    tag = (v2u){(u32)(E + 1), (u32)((E + 1) >> 32)};
    if (tid == 0) {
      *(LAS v2u*)(smem + L_TAG) = tag;
      *(LAS u32*)(smem + L_CNT) = role == 2 ? 4 : 0;
    }
    u32 D[4];
#pragma unroll
    for (int i = 0; i < 4; ++i)
      D[i] = a[i].x | (a[i].z << 4) | (c[i].x << 8) | (c[i].z << 12) | (a[i].y << 16) | (a[i].w << 20) | (c[i].y << 24) | (c[i].w << 28);
    const bool o1 = l & 1, o2 = l & 2;
    const u32 x0 = qxor1(D[0]), x1 = qxor1(D[1]), x2 = qxor1(D[2]), x3 = qxor1(D[3]);
    const u32 E0 = o1 ? x1 : D[0], E1 = o1 ? D[1] : x0, E2 = o1 ? x3 : D[2], E3 = o1 ? D[3] : x2;
    const u32 y0 = qxor2(E0), y1 = qxor2(E1), y2 = qxor2(E2), y3 = qxor2(E3);
    const u32 G0 = o2 ? y2 : E0, G2 = o2 ? E2 : y0, G1 = o2 ? y3 : E1, G3 = o2 ? E3 : y1;
    const u32 p0 = __builtin_amdgcn_perm(G1, G0, 0x06020400), p2 = __builtin_amdgcn_perm(G1, G0, 0x07030501);
    const u32 p1 = __builtin_amdgcn_perm(G3, G2, 0x06020400), p3 = __builtin_amdgcn_perm(G3, G2, 0x07030501);
    auto s02 = __builtin_amdgcn_permlane32_swap(p0, p1, false, false);
    auto s13 = __builtin_amdgcn_permlane32_swap(p2, p3, false, false);
    v4u o = {s02[0], s13[0], s02[1], s13[1]};
    ST(8);
    __builtin_amdgcn_raw_buffer_store_b128(o, rw, OFF_QP + u * 1024 + l * 16, 0, SC1);
    if (role != 0) {
      drain();
      if (l < 8) __builtin_amdgcn_raw_buffer_store_b64(tag, rw, OFF_QF + (l * 352 + u) * 128, 0, SC1);
      ST(9);
    }
  }

  if (role == 0) {
    const int rp = ra, ep = rb;
    if (w >= 4) {
      const int lw = w - 4;
      rsrc_t rx = mkrs(x, 2048 * 1024), ru = mkrs(U, 512 * 1024);
      const int vx = (rp * 128 + lw * 32 + (l >> 3)) * 1024 + (((l & 7) ^ (l >> 4)) * 16);
      const int vu = (lw * 16 + (l >> 4)) * 1024 + ep * 256 + (((l & 15) ^ ((l >> 4) << 2)) * 16);
      auto issue = [&](int s) {
        LAS char* dst = smem + (s & 3) * 32768 + lw * 4096;
#pragma unroll
        for (int a = 0; a < 4; ++a) {
          __builtin_amdgcn_raw_ptr_buffer_load_lds(ru, (LAS void*)(dst + 16384 + a * 1024), 16, (vu ^ (a * 16)) + a * 4096, s * 65536, 0, 0);
          __builtin_amdgcn_raw_ptr_buffer_load_lds(rx, (LAS void*)(dst + a * 1024), 16, (vx ^ ((a & 1) * 64)) + a * 8192, s * 128, 0, 0);
        }
      };
      issue(0); issue(1); issue(2); issue(3);
      ST(1);
      wvm<24>(); ST(8); bar();
      wvm<16>(); ST(9); bar(); issue(4);
      wvm<16>(); ST(10); bar(); issue(5);
      wvm<16>(); ST(11); bar(); issue(6);
      wvm<16>(); ST(12); bar(); issue(7);
      wvm<16>(); ST(13); bar();
      wvm<8>(); ST(14); bar();
      wvm<0>(); ST(15); bar();
      bar();
    } else {
      if (w) {
        u32 dummy;
        const u64 cp = (pc & ~(u64)255) + min((w - 1) * 64 + l, CODE_LINES - 1) * 128;
        asm volatile("global_load_dword %0, %1, off\n\ts_waitcnt vmcnt(0)" : "=v"(dummy) : "v"(cp) : "memory");
      }
      const int we = w & 1, wm = w >> 1, h = l >> 5;
      const int rowb = wm * 64 + (l & 31);
      const int xb = rowb * 128 + ((h ^ ((rowb >> 1) & 7)) * 16);
      const int q = (l & 15) >> 2, p = l & 3, blk = (l >> 4) & 1;
      const int ub = 16384 + 256 * (8 * h + q) + 16 * ((((2 * we) ^ q) << 2) | ((2 * blk + (p >> 1)) ^ (2 * h))) + 8 * (p & 1);
      v16f acc[2][2] = {};
      v4u fa[2][2], fb[2][2];
      auto rd = [&](int n, int bi) {
        const int so = ((n >> 2) & 3) * 32768, ks = n & 3;
#pragma unroll
        for (int ce = 0; ce < 2; ++ce) {
          v2u t0 = __builtin_bit_cast(v2u, __builtin_amdgcn_ds_read_tr16_b64_v4i16((LAS v4s*)(smem + ((ub ^ (ce * 64)) + so + ks * 4096))));
          v2u t1 = __builtin_bit_cast(v2u, __builtin_amdgcn_ds_read_tr16_b64_v4i16((LAS v4s*)(smem + ((ub ^ (ce * 64) ^ 16) + so + ks * 4096 + 1024))));
          fa[bi][ce] = (v4u){t0.x, t0.y, t1.x, t1.y};
          fb[bi][ce] = lds128(smem + ((xb ^ (ks * 32)) + so + ce * 4096));
        }
      };
      bar_lgkm();
      rd(0, 0);
#pragma unroll
      for (int n = 0; n < 32; ++n) {
        if (n + 1 < 32) {
          if (((n + 1) & 3) == 0) bar_lgkm();
          rd(n + 1, (n + 1) & 1);
        }
        __builtin_amdgcn_sched_barrier(0);
#pragma unroll
        for (int ce = 0; ce < 2; ++ce)
#pragma unroll
          for (int cm = 0; cm < 2; ++cm) acc[ce][cm] = mfma32(fa[n & 1][ce], fb[n & 1][cm], acc[ce][cm]);
        __builtin_amdgcn_sched_barrier(0);
        if (n == 2 && w == 0) {
          drain();
          if (l < 8) __builtin_amdgcn_raw_buffer_store_b64(tag, rw, OFF_QF + (l * 352 + u) * 128, 0, SC1);
          ST(9);
        }
      }
      bar();
      ST(1);
      const int ob = OFF_XR + (rp * 4 + wm * 2) * 32768 + (ep * 8 + we * 4) * 1024 + l * 16;
#pragma unroll
      for (int cm = 0; cm < 2; ++cm)
#pragma unroll
        for (int ce = 0; ce < 2; ++ce)
#pragma unroll
          for (int i = 0; i < 2; ++i) {
            v4u o;
#pragma unroll
            for (int t = 0; t < 4; ++t) o[t] = cvtpk(acc[ce][cm][8 * i + 2 * t], acc[ce][cm][8 * i + 2 * t + 1]);
            __builtin_amdgcn_raw_buffer_store_b128(o, rw, ob + (ce * 2 + i) * 1024, cm * 32768, SC1);
          }
      tag = *(LAS v2u*)(smem + L_TAG);
      const int fo = OFF_CF + (((rp & 3) + 4 * (l & 7)) * 8 + 2 * (rp >> 2) + ((l >> 3) & 1)) * 2048 + ep * 128;
      drain();
      u32 c = 0;
      if (l == 0) c = __hip_atomic_fetch_add((LAS u32*)(smem + L_CNT), 1u, __ATOMIC_RELAXED, __HIP_MEMORY_SCOPE_WORKGROUP);
      if (__builtin_amdgcn_readfirstlane(c) == 3 && l < 16) __builtin_amdgcn_raw_buffer_store_b64(tag, rw, fo, 0, SC1);
      ST(2);
    }
  } else if (role == 2) {
    bar_lgkm();
  } else {
    const int P = ra, g = rb;
    rsrc_t rv = mkrs(V, 1376 * 2752);
    const int kg = l >> 3, cq = l & 7;
    const int rot = g * 5 + (P & 1) * 3;
    v4u szv;
    {
      int i = (w ? (l >> 4) * 7 + w - 1 : 21) + rot;
      if (i >= 22) i -= 22;
      szv = *(const GAS v4u*)((const char*)((l & 8) ? zr : sc) + g * 2752 + min(i * 8 + cq, 171) * 16);
    }
    v2u vr[3][8];
    int un[3];
    auto vload = [&](int rd, int i) {
      i += rot;
      if (i >= 22) i -= 22;
      un[rd] = i;
      int vo = kg * (8 * 2752) + cq * 8;
      if (i == 21 && kg >= 4) vo = OOB;
#pragma unroll
      for (int e = 0; e < 8; ++e) vr[rd][e] = __builtin_amdgcn_raw_buffer_load_b64(rv, vo, (i * 64 + e) * 2752 + P * 64, 0);
    };
    const int uu = (cq & 4) | ((cq & 1) << 1) | ((cq >> 1) & 1);
    v4f cl[4] = {}, ch[4] = {};
    auto convert = [&](int rd) {
      const v4u sr = lds128(smem + L_SZ + w * 1024 + rd * 256 + kg * 16);
      const v4u zq = lds128(smem + L_SZ + w * 1024 + rd * 256 + 128 + kg * 16);
      float s[8];
#pragma unroll
      for (int t = 0; t < 4; ++t) {
        const float k = (t & 1) ? 0x1p68f : 0x1p72f;
        s[2 * t] = flo(sr[t]) * k;
        s[2 * t + 1] = fhi(sr[t]) * k;
      }
      LAS char* dst = smem + (un[rd] * 8 + kg) * 512;
#pragma unroll
      for (int c = 0; c < 4; ++c) {
        v4u o;
#pragma unroll
        for (int t = 0; t < 4; ++t) {
          const u32 a0 = vr[rd][2 * t][c >> 1], a1 = vr[rd][2 * t + 1][c >> 1];
          const float f0 = (c & 1) ? fhi(a0) : flo(a0), f1 = (c & 1) ? fhi(a1) : flo(a1);
          o[((t & 1) << 1) | (t >> 1)] = cvtpk(f0 * s[2 * t], f1 * s[2 * t + 1]);
        }
        *(LAS v4u*)(dst + (8 * c + (uu ^ (c >> 1))) * 16) = o;
        cl[c] = mfma4(zq[0], zq[2], o[0], o[1], cl[c]);
        ch[c] = mfma4(zq[1], zq[3], o[2], o[3], ch[c]);
      }
    };
    if (w) {
      vload(0, w - 1);
      vload(1, w + 6);
      vload(2, w + 13);
      __builtin_amdgcn_sched_barrier(0);
      ST(10);
      *(LAS v4u*)(smem + L_SZ + w * 1024 + l * 16) = szv;
      convert(0);
      __builtin_amdgcn_sched_barrier(0);
      ST(11);
      convert(1);
    } else {
      vload(2, 21);
      __builtin_amdgcn_sched_barrier(0);
      *(LAS v4u*)(smem + L_SZ + w * 1024 + l * 16) = szv;
      poll2(rw, OFF_QF + (xcd * 352 + g * 88 + l * 2) * 128, (1ull << 44) - 1, tag);
      __builtin_amdgcn_s_setprio(0);
    }
    ST(1);
    bar_lgkm();
    ST(2);
    const int mi = w & 3, kh = w >> 2;
    const int hh = (g * 4 + mi) * 2 + kh;
    v4u qr[11];
#pragma unroll
    for (int j = 0; j < 11; ++j) qr[j] = __builtin_amdgcn_raw_buffer_load_b128(rw, OFF_QP + hh * 11264 + l * 16, j * 1024, SC1);
    __builtin_amdgcn_sched_barrier(0);
    convert(2);
    {
      u32 cv[4];
#pragma unroll
      for (int c = 0; c < 4; ++c) cv[c] = asu(__builtin_fmaf(ch[c][0], 16.f, cl[c][0]));
      auto sa = __builtin_amdgcn_permlane16_swap(cv[0], cv[1], false, false);
      auto sb = __builtin_amdgcn_permlane16_swap(cv[2], cv[3], false, false);
      auto sd = __builtin_amdgcn_permlane32_swap(asu(asf(sa[0]) + asf(sa[1])), asu(asf(sb[0]) + asf(sb[1])), false, false);
      float v = asf(sd[0]) + asf(sd[1]);
      v += asf(__builtin_amdgcn_mov_dpp(asu(v), 0x128, 0xf, 0xf, true));
      *(LAS float*)(smem + L_CB + w * 128 + (l & 7) * 16 + (l >> 4) * 4) = v * 0x1p-72f;
    }
    ST(12);
    bar_lgkm();
    ST(13);
    const int sl = l & 31;
    v16f acc = {};
    LAS char* bsrc = smem + kh * 45056 + (l >> 5) * 512 + (8 * (sl & 3) + ((sl >> 2) ^ ((sl & 3) >> 1))) * 16;
    u32 m0 = 0x000F000Fu, m1 = 0x00F000F0u;
    asm volatile("" : "+v"(m0), "+v"(m1));
    auto step = [&](int s, v4u bfr) {
      const u32 qv = qr[s >> 2][s & 3], qh = qv >> 8;
      acc = mfma32((v4u){qv & m0, qh & m0, qv & m1, qh & m1}, bfr, acc);
    };
    v4u bf[4];
#pragma unroll
    for (int s = 0; s < 3; ++s) bf[s] = lds128(bsrc + s * 1024);
#pragma unroll
    for (int s = 0; s < 42; ++s) {
      if (s + 3 < 44) bf[(s + 3) & 3] = lds128(bsrc + (s + 3) * 1024);
      step(s, bf[s & 3]);
      __builtin_amdgcn_sched_barrier(0);
    }
    float cc = 0.f;
    if (!kh) {
      step(42, bf[42 & 3]);
      step(43, bf[43 & 3]);
#pragma unroll
      for (int j = 0; j < 4; ++j)
        *(LAS v4f*)(smem + L_RED + mi * 4096 + j * 1024 + l * 16) = (v4f){acc[4 * j], acc[4 * j + 1], acc[4 * j + 2], acc[4 * j + 3]};
    } else {
      const int u2 = sl >> 2;
      const int col = 4 * ((u2 & 4) | ((u2 & 1) << 1) | ((u2 >> 1) & 1)) + (sl & 3);
#pragma unroll
      for (int j = 0; j < 8; ++j) cc -= *(LAS float*)(smem + L_CB + j * 128 + col * 4);
    }
    bar_lgkm();
    ST(3);
    if (kh) {
      v4f o4[4];
#pragma unroll
      for (int j = 0; j < 4; ++j) o4[j] = *(LAS v4f*)(smem + L_RED + mi * 4096 + j * 1024 + l * 16);
#pragma unroll
      for (int i = 0; i < 2; ++i) {
        v4u o;
#pragma unroll
        for (int t = 0; t < 4; ++t) {
          const int j = 8 * i + 2 * t;
          o[t] = cvtpk(__builtin_fmaf(acc[j] + o4[j >> 2][j & 3], 0x1p61f, cc), __builtin_fmaf(acc[j + 1] + o4[j >> 2][(j & 3) + 1], 0x1p61f, cc));
        }
        __builtin_amdgcn_raw_buffer_store_b128(o, rw, OFF_T + P * 32768 + (g * 8 + mi * 2 + i) * 1024 + l * 16, 0, SC1);
      }
      tag = *(LAS v2u*)(smem + L_TAG);
      const int ct = P / 3, pj = P - 3 * ct;
      const int fo = OFF_CF + (((l & 3) + 4 * (ct & 7)) * 8 + 2 * ((l >> 2) & 3) + (ct >> 3)) * 2048 + (4 + g * 3 + pj) * 128;
      drain();
      u32 c = 0;
      if (l == 0) c = __hip_atomic_fetch_add((LAS u32*)(smem + L_CNT), 1u, __ATOMIC_RELAXED, __HIP_MEMORY_SCOPE_WORKGROUP);
      if (__builtin_amdgcn_readfirstlane(c) == 3 && l < 16) __builtin_amdgcn_raw_buffer_store_b64(tag, rw, fo, 0, SC1);
    }
  }
  __builtin_amdgcn_s_setprio(0);
  {
    const int rp = (xcd >> 1) * 4 + (r & 3), ct = (xcd & 1) * 8 + (r >> 2);
    if (ct < 15) {
      tag = *(LAS v2u*)(smem + L_TAG);
      if (w >= 4) {
        const int g = w - 4;
        ST(4);
        poll(rw, OFF_CF + b * 2048 + (4 + g * 3 + l) * 128, ct == 14 ? 1 : 7, tag);
        ST(5);
        v4u t[8][3];
        const int tb = OFF_T + g * 8192 + l * 16;
        const int p0 = 3 * ct * 32768, p1 = min(3 * ct + 1, 42) * 32768, p2 = min(3 * ct + 2, 42) * 32768;
#pragma unroll
        for (int c = 0; c < 8; ++c) {
          t[c][0] = __builtin_amdgcn_raw_buffer_load_b128(rw, tb + (c & 3) * 1024 + (c >> 2) * 4096, p0, SC1);
          t[c][1] = __builtin_amdgcn_raw_buffer_load_b128(rw, tb + (c & 3) * 1024 + (c >> 2) * 4096, p1, SC1);
          t[c][2] = __builtin_amdgcn_raw_buffer_load_b128(rw, tb + (c & 3) * 1024 + (c >> 2) * 4096, p2, SC1);
        }
        __builtin_amdgcn_sched_barrier(0);
#pragma unroll
        for (int c = 0; c < 8; ++c) {
#pragma unroll
          for (int pj = 0; pj < 3; ++pj) *(LAS v4u*)(smem + ((c * 4 + g) * 3 + pj) * 1024 + l * 16) = t[c][pj];
          bar_lgkm();
        }
        if (b == 0 && tid == 256) __builtin_amdgcn_raw_buffer_store_b64(tag, rw, 0, 0, SC1);
        ST(6);
      } else {
        ST(4);
        poll(rw, OFF_CF + b * 2048 + l * 128, 15, tag);
        ST(5);
        v4u X[32];
        const int xb = OFF_XR + (rp * 4 + w) * 32768 + l * 16;
#pragma unroll
        for (int n = 0; n < 32; ++n) {
          if (n == NX1) {
#pragma unroll 1
            for (int it = 0; it < 4096; ++it) {
              if (__builtin_amdgcn_readfirstlane(*(volatile LAS u32*)(smem + L_CNT)) >= 4) break;
              __builtin_amdgcn_s_sleep(1);
            }
          }
          X[n] = __builtin_amdgcn_raw_buffer_load_b128(rw, xb + ((n >> 2) & 3) * 1024, (n & 3) * 8192 + (n >> 4) * 4096, SC1);
        }
        ST(14);
        v16f acc[3] = {};
        v4u fa[3][3];
        LAS char* ab = smem + l * 16;
        bar();
        ST(15);
#pragma unroll
        for (int n = 0; n < 2; ++n)
#pragma unroll
          for (int pj = 0; pj < 3; ++pj) fa[n][pj] = lds128(ab + (n * 3 + pj) * 1024);
#pragma unroll
        for (int n = 0; n < 32; ++n) {
          if (n + 2 < 32) {
            if (((n + 2) & 3) == 0) bar();
#pragma unroll
            for (int pj = 0; pj < 3; ++pj) fa[(n + 2) % 3][pj] = lds128(ab + ((n + 2) * 3 + pj) * 1024);
          }
          __builtin_amdgcn_sched_barrier(0);
#pragma unroll
          for (int pj = 0; pj < 3; ++pj) acc[pj] = mfma32(fa[n % 3][pj], X[n], acc[pj]);
          __builtin_amdgcn_sched_barrier(0);
        }
        ST(6);
        LAS char* eb = smem + L_EP + w * (32 * 208);
#pragma unroll
        for (int pj = 0; pj < 3; ++pj)
#pragma unroll
          for (int i = 0; i < 2; ++i) {
            v4u o;
#pragma unroll
            for (int t = 0; t < 4; ++t) o[t] = cvtpk(acc[pj][8 * i + 2 * t], acc[pj][8 * i + 2 * t + 1]);
            *(LAS v4u*)(eb + (l & 31) * 208 + (pj * 4 + 2 * i + (l >> 5)) * 16) = o;
          }
        asm volatile("s_waitcnt lgkmcnt(0)" ::: "memory");
        rsrc_t ry = mkrs(y, 2048 * 2752);
        const int yb = (rp * 128 + w * 32) * 2752 + ct * 192;
#pragma unroll
        for (int t = 0; t < 6; ++t) {
          const int idx = t * 64 + l, row = idx / 12, ch = idx - row * 12;
          v4u o = lds128(eb + row * 208 + ch * 16);
          const int vo = (ct == 14 && ch >= 4) ? OOB : row * 2752 + ch * 16;
          __builtin_amdgcn_raw_buffer_store_b128(o, ry, vo, yb, YAUX);
        }
      }
    }
  }
}

extern "C" void kernel_launch(void* const* d_in, const int* in_sizes, int n_in, void* d_out, int out_size, void* d_ws,
                              size_t ws_size, hipStream_t stream) {
  (void)hipFuncSetAttribute((const void*)kmain, hipFuncAttributeMaxDynamicSharedMemorySize, LDS_BYTES);
  kmain<<<256, 512, LDS_BYTES, stream>>>((const u16*)d_in[0], (const int*)d_in[1], (const u16*)d_in[2], (const u16*)d_in[3],
                                         (const u16*)d_in[4], (const u16*)d_in[5], (u16*)d_out, (char*)d_ws);
}
```

```cpp
#define STAMP
#define RA 1
#define WA 0
#define IA 0
#define SA 2
#define OA 0
#define RB 1
#define WB 0
#define IB 8
#define SB 1
#define OB 0
#include <hip/hip_runtime.h>
#include <stdint.h>

typedef unsigned u32;
typedef unsigned long long u64;
typedef unsigned short u16;
typedef u32 v4u __attribute__((ext_vector_type(4)));
typedef u32 v2u __attribute__((ext_vector_type(2)));
typedef float v4f __attribute__((ext_vector_type(4)));
typedef float v16f __attribute__((ext_vector_type(16)));
typedef __bf16 v8bf __attribute__((ext_vector_type(8)));
typedef __bf16 v2bf __attribute__((ext_vector_type(2)));
typedef float v2f __attribute__((ext_vector_type(2)));
typedef short v4s __attribute__((ext_vector_type(4)));
#define LAS __attribute__((address_space(3)))
#define CAS __attribute__((address_space(4)))
#define GAS __attribute__((address_space(1)))
typedef __amdgpu_buffer_rsrc_t rsrc_t;

#define YAUX 16
#define NX1 8
#define SPREAD 1
#define CODEPF 1

constexpr int OFF_QF = 6 << 20, OFF_CF = 7 << 20;
constexpr int OFF_QP = 65536, OFF_XR = 1 << 20, OFF_T = 4 << 20, WS_BYTES = 8 << 20;
constexpr int CODE_LINES = 144;
constexpr int SC1 = 16;
constexpr int OOB = 0x40000000;
constexpr int L_RED = 98304, L_CB = 114688, L_SZ = 116736, L_EP = 98304, L_CNT = 131072, L_TAG = 131080, LDS_BYTES = 131072 + 64;

__device__ __forceinline__ rsrc_t mkrs(const void* p, int bytes) {
  return __builtin_amdgcn_make_buffer_rsrc((void*)p, (short)0, bytes, 0x00020000);
}
__device__ __forceinline__ v16f mfma32(v4u a, v4u b, v16f c) {
  return __builtin_amdgcn_mfma_f32_32x32x16_bf16(__builtin_bit_cast(v8bf, a), __builtin_bit_cast(v8bf, b), c, 0, 0, 0);
}
__device__ __forceinline__ v4f mfma4(u32 a0, u32 a1, u32 b0, u32 b1, v4f c) {
  return __builtin_amdgcn_mfma_f32_4x4x4bf16_1k(__builtin_bit_cast(v4s, (v2u){a0, a1}), __builtin_bit_cast(v4s, (v2u){b0, b1}), c, 0, 0, 0);
}
__device__ __forceinline__ u32 cvtpk(float a, float b) {
  v2f f = {a, b};
  return __builtin_bit_cast(u32, __builtin_convertvector(f, v2bf));
}
__device__ __forceinline__ float asf(u32 v) { return __builtin_bit_cast(float, v); }
__device__ __forceinline__ u32 asu(float v) { return __builtin_bit_cast(u32, v); }
__device__ __forceinline__ float flo(u32 v) { return asf(v << 16); }
__device__ __forceinline__ float fhi(u32 v) { return asf(v & 0xffff0000u); }
__device__ __forceinline__ void bar() { asm volatile("s_barrier" ::: "memory"); }
__device__ __forceinline__ void bar_lgkm() { asm volatile("s_waitcnt lgkmcnt(0)\n\ts_barrier" ::: "memory"); }
__device__ __forceinline__ void drain() { asm volatile("s_waitcnt vmcnt(0)" ::: "memory"); }
template <int N> __device__ __forceinline__ void wvm() { asm volatile("s_waitcnt vmcnt(%0)" ::"n"(N) : "memory"); }
__device__ __forceinline__ v4u lds128(LAS char* p) { return *(LAS v4u*)p; }

__device__ __forceinline__ void poll(rsrc_t rw, int off, u64 mask, v2u tag) {
  v2u f;
  u64 sv;
  u32 cnt;
  asm volatile(
      "s_mov_b64 %1, exec\n\ts_mov_b64 exec, %5\n\ts_mov_b32 %2, 0x10000\n"
      "1:\n\tbuffer_load_dwordx2 %0, %3, %4, 0 offen sc1\n\ts_waitcnt vmcnt(0)\n\t"
      "v_cmp_ne_u64 vcc, %0, %6\n\ts_cbranch_vccz 2f\n\ts_sleep 1\n\ts_sub_u32 %2, %2, 1\n\ts_cbranch_scc0 1b\n"
      "2:\n\ts_mov_b64 exec, %1"
      : "=&v"(f), "=&s"(sv), "=&s"(cnt)
      : "v"(off), "s"(rw), "s"(mask), "v"(tag)
      : "vcc", "scc", "memory");
}
__device__ __forceinline__ void poll2(rsrc_t rw, int off, u64 mask, v2u tag) {
  v2u f, h;
  u64 sv, m2;
  u32 cnt;
  asm volatile(
      "s_mov_b64 %1, exec\n\ts_mov_b64 exec, %7\n\ts_mov_b32 %2, 0x10000\n"
      "1:\n\tbuffer_load_dwordx2 %0, %5, %6, 0 offen sc1\n\tbuffer_load_dwordx2 %3, %5, %6, 0 offen offset:128 sc1\n\ts_waitcnt vmcnt(0)\n\t"
      "v_cmp_ne_u64 vcc, %0, %8\n\tv_cmp_ne_u64 %4, %3, %8\n\ts_or_b64 %4, %4, vcc\n\ts_cbranch_scc0 2f\n\ts_sleep 1\n\ts_sub_u32 %2, %2, 1\n\ts_cbranch_scc0 1b\n"
      "2:\n\ts_mov_b64 exec, %1"
      : "=&v"(f), "=&s"(sv), "=&s"(cnt), "=&v"(h), "=&s"(m2)
      : "v"(off), "s"(rw), "s"(mask), "v"(tag)
      : "vcc", "scc", "memory");
}

#define ST(i) st[i] = (u32)__builtin_amdgcn_s_memrealtime()

__device__ __forceinline__ u32 qxor1(u32 v) { return __builtin_amdgcn_mov_dpp(v, 0xB1, 0xf, 0xf, true); }
__device__ __forceinline__ u32 qxor2(u32 v) { return __builtin_amdgcn_mov_dpp(v, 0x4E, 0xf, 0xf, true); }

__global__ __launch_bounds__(512, 2) void kmain(const u16* x, const int* qw, const u16* sc, const u16* zr, const u16* U,
                                                const u16* V, u16* y, char* ws) {
  extern __shared__ __attribute__((aligned(16))) char smem_[];
  LAS char* smem = (LAS char*)smem_;
  u32 st[16] = {};
  u64 c0_ = __builtin_amdgcn_s_memtime();
  ST(0);
  u64 pc;
  asm volatile("s_getpc_b64 %0" : "=s"(pc));
  asm volatile("" : "+s"(x), "+s"(qw), "+s"(sc), "+s"(zr), "+s"(U), "+s"(V), "+s"(y), "+s"(ws));
  const int b = blockIdx.x, tid = threadIdx.x;
  const int w = __builtin_amdgcn_readfirstlane(tid >> 6), l = tid & 63;
  const int xcd = b & 7, r = b >> 3;
  rsrc_t rw = mkrs(ws, WS_BYTES);
  int role, ra, rb, u;
  {
    const int j = xcd < 3 ? r : r - 4;
    role = j < 0 ? 2 : j < 8 ? 0 : 1;
    rb = j & 3;
    ra = role == 0 ? 2 * xcd + (j >> 2) : (xcd < 3 ? 6 * xcd : 3 + 5 * xcd) + (j >> 2) - 2;
    u = role == 2 ? 236 + ((xcd - 3) * 4 + r) * 6 + w : role == 0 ? 172 + ra * 4 + rb : ra * 4 + rb;
  }
  v2u tag;
  if (role == 2 ? (w < 6 && u < 352) : w == 0) {
    __builtin_amdgcn_s_setprio(3);
    const u64 E = *(const CAS u64*)ws;
    rsrc_t rq = mkrs(qw, 512 * 5504);
    const int rt = u / 22, un = u - rt * 22;
    int voff = (rt * 32 + (l & 28)) * 5504 + un * 256 + (l >> 5) * 128 + (l & 3) * 16;
    if (un == 21 && l >= 32) voff = OOB;
    v4u a[4], c[4];
#pragma unroll
    for (int i = 0; i < 4; ++i) {
      a[i] = __builtin_amdgcn_raw_buffer_load_b128(rq, voff, i * 5504, 0);
      c[i] = __builtin_amdgcn_raw_buffer_load_b128(rq, voff + 64, i * 5504, 0);
    }
    __builtin_amdgcn_sched_barrier(0);
    tag = (v2u){(u32)(E + 1), (u32)((E + 1) >> 32)};
    if (tid == 0) {
      *(LAS v2u*)(smem + L_TAG) = tag;
      *(LAS u32*)(smem + L_CNT) = role == 2 ? 4 : 0;
    }
    u32 D[4];
#pragma unroll
    for (int i = 0; i < 4; ++i)
      D[i] = a[i].x | (a[i].z << 4) | (c[i].x << 8) | (c[i].z << 12) | (a[i].y << 16) | (a[i].w << 20) | (c[i].y << 24) | (c[i].w << 28);
    const bool o1 = l & 1, o2 = l & 2;
    const u32 x0 = qxor1(D[0]), x1 = qxor1(D[1]), x2 = qxor1(D[2]), x3 = qxor1(D[3]);
    const u32 E0 = o1 ? x1 : D[0], E1 = o1 ? D[1] : x0, E2 = o1 ? x3 : D[2], E3 = o1 ? D[3] : x2;
    const u32 y0 = qxor2(E0), y1 = qxor2(E1), y2 = qxor2(E2), y3 = qxor2(E3);
    const u32 G0 = o2 ? y2 : E0, G2 = o2 ? E2 : y0, G1 = o2 ? y3 : E1, G3 = o2 ? E3 : y1;
    const u32 p0 = __builtin_amdgcn_perm(G1, G0, 0x06020400), p2 = __builtin_amdgcn_perm(G1, G0, 0x07030501);
    const u32 p1 = __builtin_amdgcn_perm(G3, G2, 0x06020400), p3 = __builtin_amdgcn_perm(G3, G2, 0x07030501);
    auto s02 = __builtin_amdgcn_permlane32_swap(p0, p1, false, false);
    auto s13 = __builtin_amdgcn_permlane32_swap(p2, p3, false, false);
    v4u o = {s02[0], s13[0], s02[1], s13[1]};
    ST(8);
    __builtin_amdgcn_raw_buffer_store_b128(o, rw, OFF_QP + u * 1024 + l * 16, 0, SC1);
    if (role != 0) {
      drain();
      if (l < 8) __builtin_amdgcn_raw_buffer_store_b64(tag, rw, OFF_QF + (l * 352 + u) * 128, 0, SC1);
      ST(9);
    }
  }

  if (role == 0) {
    const int rp = ra, ep = rb;
    if (w >= 4) {
      const int lw = w - 4;
      rsrc_t rx = mkrs(x, 2048 * 1024), ru = mkrs(U, 512 * 1024);
      const int vx = (rp * 128 + lw * 32 + (l >> 3)) * 1024 + (((l & 7) ^ (l >> 4)) * 16);
      const int vu = (lw * 16 + (l >> 4)) * 1024 + ep * 256 + (((l & 15) ^ ((l >> 4) << 2)) * 16);
      auto issue = [&](int s) {
        LAS char* dst = smem + (s & 3) * 32768 + lw * 4096;
#pragma unroll
        for (int a = 0; a < 4; ++a) {
          __builtin_amdgcn_raw_ptr_buffer_load_lds(ru, (LAS void*)(dst + 16384 + a * 1024), 16, (vu ^ (a * 16)) + a * 4096, s * 65536, 0, 0);
          __builtin_amdgcn_raw_ptr_buffer_load_lds(rx, (LAS void*)(dst + a * 1024), 16, (vx ^ ((a & 1) * 64)) + a * 8192, s * 128, 0, 0);
        }
      };
      issue(0); issue(1); issue(2); issue(3);
      ST(1);
      wvm<24>(); ST(8); bar();
      wvm<16>(); ST(9); bar(); issue(4);
      wvm<16>(); ST(10); bar(); issue(5);
      wvm<16>(); ST(11); bar(); issue(6);
      wvm<16>(); ST(12); bar(); issue(7);
      wvm<16>(); ST(13); bar();
      wvm<8>(); ST(14); bar();
      wvm<0>(); ST(15); bar();
      bar();
    } else {
      if (w) {
        u32 dummy;
        const u64 cp = (pc & ~(u64)255) + min((w - 1) * 64 + l, CODE_LINES - 1) * 128;
        asm volatile("global_load_dword %0, %1, off\n\ts_waitcnt vmcnt(0)" : "=v"(dummy) : "v"(cp) : "memory");
      }
      const int we = w & 1, wm = w >> 1, h = l >> 5;
      const int rowb = wm * 64 + (l & 31);
      const int xb = rowb * 128 + ((h ^ ((rowb >> 1) & 7)) * 16);
      const int q = (l & 15) >> 2, p = l & 3, blk = (l >> 4) & 1;
      const int ub = 16384 + 256 * (8 * h + q) + 16 * ((((2 * we) ^ q) << 2) | ((2 * blk + (p >> 1)) ^ (2 * h))) + 8 * (p & 1);
      v16f acc[2][2] = {};
      v4u fa[2][2], fb[2][2];
      auto rd = [&](int n, int bi) {
        const int so = ((n >> 2) & 3) * 32768, ks = n & 3;
#pragma unroll
        for (int ce = 0; ce < 2; ++ce) {
          v2u t0 = __builtin_bit_cast(v2u, __builtin_amdgcn_ds_read_tr16_b64_v4i16((LAS v4s*)(smem + ((ub ^ (ce * 64)) + so + ks * 4096))));
          v2u t1 = __builtin_bit_cast(v2u, __builtin_amdgcn_ds_read_tr16_b64_v4i16((LAS v4s*)(smem + ((ub ^ (ce * 64) ^ 16) + so + ks * 4096 + 1024))));
          fa[bi][ce] = (v4u){t0.x, t0.y, t1.x, t1.y};
          fb[bi][ce] = lds128(smem + ((xb ^ (ks * 32)) + so + ce * 4096));
        }
      };
      bar_lgkm();
      rd(0, 0);
#pragma unroll
      for (int n = 0; n < 32; ++n) {
        if (n + 1 < 32) {
          if (((n + 1) & 3) == 0) bar_lgkm();
          rd(n + 1, (n + 1) & 1);
        }
        __builtin_amdgcn_sched_barrier(0);
#pragma unroll
        for (int ce = 0; ce < 2; ++ce)
#pragma unroll
          for (int cm = 0; cm < 2; ++cm) acc[ce][cm] = mfma32(fa[n & 1][ce], fb[n & 1][cm], acc[ce][cm]);
        __builtin_amdgcn_sched_barrier(0);
        if (n == 2 && w == 0) {
          drain();
          if (l < 8) __builtin_amdgcn_raw_buffer_store_b64(tag, rw, OFF_QF + (l * 352 + u) * 128, 0, SC1);
          ST(9);
        }
      }
      bar();
      ST(1);
      const int ob = OFF_XR + (rp * 4 + wm * 2) * 32768 + (ep * 8 + we * 4) * 1024 + l * 16;
#pragma unroll
      for (int cm = 0; cm < 2; ++cm)
#pragma unroll
        for (int ce = 0; ce < 2; ++ce)
#pragma unroll
          for (int i = 0; i < 2; ++i) {
            v4u o;
#pragma unroll
            for (int t = 0; t < 4; ++t) o[t] = cvtpk(acc[ce][cm][8 * i + 2 * t], acc[ce][cm][8 * i + 2 * t + 1]);
            __builtin_amdgcn_raw_buffer_store_b128(o, rw, ob + (ce * 2 + i) * 1024, cm * 32768, SC1);
          }
      tag = *(LAS v2u*)(smem + L_TAG);
      const int fo = OFF_CF + (((rp & 3) + 4 * (l & 7)) * 8 + 2 * (rp >> 2) + ((l >> 3) & 1)) * 2048 + ep * 128;
      drain();
      u32 c = 0;
      if (l == 0) c = __hip_atomic_fetch_add((LAS u32*)(smem + L_CNT), 1u, __ATOMIC_RELAXED, __HIP_MEMORY_SCOPE_WORKGROUP);
      if (__builtin_amdgcn_readfirstlane(c) == 3 && l < 16) __builtin_amdgcn_raw_buffer_store_b64(tag, rw, fo, 0, SC1);
      ST(2);
    }
  } else if (role == 2) {
    bar_lgkm();
  } else {
    const int P = ra, g = rb;
    rsrc_t rv = mkrs(V, 1376 * 2752);
    const int kg = l >> 3, cq = l & 7;
    const int rot = g * 5 + (P & 1) * 3;
    v4u szv;
    {
      int i = (w ? (l >> 4) * 7 + w - 1 : 21) + rot;
      if (i >= 22) i -= 22;
      szv = *(const GAS v4u*)((const char*)((l & 8) ? zr : sc) + g * 2752 + min(i * 8 + cq, 171) * 16);
    }
    v2u vr[3][8];
    int un[3];
    auto vload = [&](int rd, int i) {
      i += rot;
      if (i >= 22) i -= 22;
      un[rd] = i;
      int vo = kg * (8 * 2752) + cq * 8;
      if (i == 21 && kg >= 4) vo = OOB;
#pragma unroll
      for (int e = 0; e < 8; ++e) vr[rd][e] = __builtin_amdgcn_raw_buffer_load_b64(rv, vo, (i * 64 + e) * 2752 + P * 64, 0);
    };
    const int uu = (cq & 4) | ((cq & 1) << 1) | ((cq >> 1) & 1);
    v4f cl[4] = {}, ch[4] = {};
    auto convert = [&](int rd) {
      const v4u sr = lds128(smem + L_SZ + w * 1024 + rd * 256 + kg * 16);
      const v4u zq = lds128(smem + L_SZ + w * 1024 + rd * 256 + 128 + kg * 16);
      float s[8];
#pragma unroll
      for (int t = 0; t < 4; ++t) {
        const float k = (t & 1) ? 0x1p68f : 0x1p72f;
        s[2 * t] = flo(sr[t]) * k;
        s[2 * t + 1] = fhi(sr[t]) * k;
      }
      LAS char* dst = smem + (un[rd] * 8 + kg) * 512;
#pragma unroll
      for (int c = 0; c < 4; ++c) {
        v4u o;
#pragma unroll
        for (int t = 0; t < 4; ++t) {
          const u32 a0 = vr[rd][2 * t][c >> 1], a1 = vr[rd][2 * t + 1][c >> 1];
          const float f0 = (c & 1) ? fhi(a0) : flo(a0), f1 = (c & 1) ? fhi(a1) : flo(a1);
          o[((t & 1) << 1) | (t >> 1)] = cvtpk(f0 * s[2 * t], f1 * s[2 * t + 1]);
        }
        *(LAS v4u*)(dst + (8 * c + (uu ^ (c >> 1))) * 16) = o;
        cl[c] = mfma4(zq[0], zq[2], o[0], o[1], cl[c]);
        ch[c] = mfma4(zq[1], zq[3], o[2], o[3], ch[c]);
      }
    };
    if (w) {
      vload(0, w - 1);
      vload(1, w + 6);
      vload(2, w + 13);
      __builtin_amdgcn_sched_barrier(0);
      ST(10);
      *(LAS v4u*)(smem + L_SZ + w * 1024 + l * 16) = szv;
      convert(0);
      __builtin_amdgcn_sched_barrier(0);
      ST(11);
      convert(1);
    } else {
      vload(2, 21);
      __builtin_amdgcn_sched_barrier(0);
      *(LAS v4u*)(smem + L_SZ + w * 1024 + l * 16) = szv;
      poll2(rw, OFF_QF + (xcd * 352 + g * 88 + l * 2) * 128, (1ull << 44) - 1, tag);
      __builtin_amdgcn_s_setprio(0);
    }
    ST(1);
    bar_lgkm();
    ST(2);
    const int mi = w & 3, kh = w >> 2;
    const int hh = (g * 4 + mi) * 2 + kh;
    v4u qr[11];
#pragma unroll
    for (int j = 0; j < 11; ++j) qr[j] = __builtin_amdgcn_raw_buffer_load_b128(rw, OFF_QP + hh * 11264 + l * 16, j * 1024, SC1);
    __builtin_amdgcn_sched_barrier(0);
    convert(2);
    {
      u32 cv[4];
#pragma unroll
      for (int c = 0; c < 4; ++c) cv[c] = asu(__builtin_fmaf(ch[c][0], 16.f, cl[c][0]));
      auto sa = __builtin_amdgcn_permlane16_swap(cv[0], cv[1], false, false);
      auto sb = __builtin_amdgcn_permlane16_swap(cv[2], cv[3], false, false);
      auto sd = __builtin_amdgcn_permlane32_swap(asu(asf(sa[0]) + asf(sa[1])), asu(asf(sb[0]) + asf(sb[1])), false, false);
      float v = asf(sd[0]) + asf(sd[1]);
      v += asf(__builtin_amdgcn_mov_dpp(asu(v), 0x128, 0xf, 0xf, true));
      *(LAS float*)(smem + L_CB + w * 128 + (l & 7) * 16 + (l >> 4) * 4) = v * 0x1p-72f;
    }
    ST(12);
    bar_lgkm();
    ST(13);
    const int sl = l & 31;
    v16f acc = {};
    LAS char* bsrc = smem + kh * 45056 + (l >> 5) * 512 + (8 * (sl & 3) + ((sl >> 2) ^ ((sl & 3) >> 1))) * 16;
    u32 m0 = 0x000F000Fu, m1 = 0x00F000F0u;
    asm volatile("" : "+v"(m0), "+v"(m1));
    auto step = [&](int s, v4u bfr) {
      const u32 qv = qr[s >> 2][s & 3], qh = qv >> 8;
      acc = mfma32((v4u){qv & m0, qh & m0, qv & m1, qh & m1}, bfr, acc);
    };
    v4u bf[4];
#pragma unroll
    for (int s = 0; s < 3; ++s) bf[s] = lds128(bsrc + s * 1024);
#pragma unroll
    for (int s = 0; s < 42; ++s) {
      if (s + 3 < 44) bf[(s + 3) & 3] = lds128(bsrc + (s + 3) * 1024);
      step(s, bf[s & 3]);
      __builtin_amdgcn_sched_barrier(0);
    }
    float cc = 0.f;
    if (!kh) {
      step(42, bf[42 & 3]);
      step(43, bf[43 & 3]);
#pragma unroll
      for (int j = 0; j < 4; ++j)
        *(LAS v4f*)(smem + L_RED + mi * 4096 + j * 1024 + l * 16) = (v4f){acc[4 * j], acc[4 * j + 1], acc[4 * j + 2], acc[4 * j + 3]};
    } else {
      const int u2 = sl >> 2;
      const int col = 4 * ((u2 & 4) | ((u2 & 1) << 1) | ((u2 >> 1) & 1)) + (sl & 3);
#pragma unroll
      for (int j = 0; j < 8; ++j) cc -= *(LAS float*)(smem + L_CB + j * 128 + col * 4);
    }
    bar_lgkm();
    ST(3);
    if (kh) {
      v4f o4[4];
#pragma unroll
      for (int j = 0; j < 4; ++j) o4[j] = *(LAS v4f*)(smem + L_RED + mi * 4096 + j * 1024 + l * 16);
#pragma unroll
      for (int i = 0; i < 2; ++i) {
        v4u o;
#pragma unroll
        for (int t = 0; t < 4; ++t) {
          const int j = 8 * i + 2 * t;
          o[t] = cvtpk(__builtin_fmaf(acc[j] + o4[j >> 2][j & 3], 0x1p61f, cc), __builtin_fmaf(acc[j + 1] + o4[j >> 2][(j & 3) + 1], 0x1p61f, cc));
        }
        __builtin_amdgcn_raw_buffer_store_b128(o, rw, OFF_T + P * 32768 + (g * 8 + mi * 2 + i) * 1024 + l * 16, 0, SC1);
      }
      tag = *(LAS v2u*)(smem + L_TAG);
      const int ct = P / 3, pj = P - 3 * ct;
      const int fo = OFF_CF + (((l & 3) + 4 * (ct & 7)) * 8 + 2 * ((l >> 2) & 3) + (ct >> 3)) * 2048 + (4 + g * 3 + pj) * 128;
      drain();
      u32 c = 0;
      if (l == 0) c = __hip_atomic_fetch_add((LAS u32*)(smem + L_CNT), 1u, __ATOMIC_RELAXED, __HIP_MEMORY_SCOPE_WORKGROUP);
      if (__builtin_amdgcn_readfirstlane(c) == 3 && l < 16) __builtin_amdgcn_raw_buffer_store_b64(tag, rw, fo, 0, SC1);
    }
  }
  __builtin_amdgcn_s_setprio(0);
  {
    const int rp = (xcd >> 1) * 4 + (r & 3), ct = (xcd & 1) * 8 + (r >> 2);
    if (ct < 15) {
      tag = *(LAS v2u*)(smem + L_TAG);
      if (w >= 4) {
        const int g = w - 4;
        ST(4);
        poll(rw, OFF_CF + b * 2048 + (4 + g * 3 + l) * 128, ct == 14 ? 1 : 7, tag);
        ST(5);
        v4u t[8][3];
        const int tb = OFF_T + g * 8192 + l * 16;
        const int p0 = 3 * ct * 32768, p1 = min(3 * ct + 1, 42) * 32768, p2 = min(3 * ct + 2, 42) * 32768;
#pragma unroll
        for (int c = 0; c < 8; ++c) {
          t[c][0] = __builtin_amdgcn_raw_buffer_load_b128(rw, tb + (c & 3) * 1024 + (c >> 2) * 4096, p0, SC1);
          t[c][1] = __builtin_amdgcn_raw_buffer_load_b128(rw, tb + (c & 3) * 1024 + (c >> 2) * 4096, p1, SC1);
          t[c][2] = __builtin_amdgcn_raw_buffer_load_b128(rw, tb + (c & 3) * 1024 + (c >> 2) * 4096, p2, SC1);
        }
        __builtin_amdgcn_sched_barrier(0);
#pragma unroll
        for (int c = 0; c < 8; ++c) {
#pragma unroll
          for (int pj = 0; pj < 3; ++pj) *(LAS v4u*)(smem + ((c * 4 + g) * 3 + pj) * 1024 + l * 16) = t[c][pj];
          bar_lgkm();
        }
        if (b == 0 && tid == 256) __builtin_amdgcn_raw_buffer_store_b64(tag, rw, 0, 0, SC1);
        ST(6);
      } else {
        ST(4);
        poll(rw, OFF_CF + b * 2048 + l * 128, 15, tag);
        ST(5);
        v4u X[32];
        const int xb = OFF_XR + (rp * 4 + w) * 32768 + l * 16;
#pragma unroll
        for (int n = 0; n < 32; ++n) {
          if (n == NX1) {
#pragma unroll 1
            for (int it = 0; it < 4096; ++it) {
              if (__builtin_amdgcn_readfirstlane(*(volatile LAS u32*)(smem + L_CNT)) >= 4) break;
              __builtin_amdgcn_s_sleep(1);
            }
          }
          X[n] = __builtin_amdgcn_raw_buffer_load_b128(rw, xb + ((n >> 2) & 3) * 1024, (n & 3) * 8192 + (n >> 4) * 4096, SC1);
        }
        ST(14);
        v16f acc[3] = {};
        v4u fa[3][3];
        LAS char* ab = smem + l * 16;
        bar();
        ST(15);
#pragma unroll
        for (int n = 0; n < 2; ++n)
#pragma unroll
          for (int pj = 0; pj < 3; ++pj) fa[n][pj] = lds128(ab + (n * 3 + pj) * 1024);
#pragma unroll
        for (int n = 0; n < 32; ++n) {
          if (n + 2 < 32) {
            if (((n + 2) & 3) == 0) bar();
#pragma unroll
            for (int pj = 0; pj < 3; ++pj) fa[(n + 2) % 3][pj] = lds128(ab + ((n + 2) * 3 + pj) * 1024);
          }
          __builtin_amdgcn_sched_barrier(0);
#pragma unroll
          for (int pj = 0; pj < 3; ++pj) acc[pj] = mfma32(fa[n % 3][pj], X[n], acc[pj]);
          __builtin_amdgcn_sched_barrier(0);
        }
        ST(6);
        LAS char* eb = smem + L_EP + w * (32 * 208);
#pragma unroll
        for (int pj = 0; pj < 3; ++pj)
#pragma unroll
          for (int i = 0; i < 2; ++i) {
            v4u o;
#pragma unroll
            for (int t = 0; t < 4; ++t) o[t] = cvtpk(acc[pj][8 * i + 2 * t], acc[pj][8 * i + 2 * t + 1]);
            *(LAS v4u*)(eb + (l & 31) * 208 + (pj * 4 + 2 * i + (l >> 5)) * 16) = o;
          }
        asm volatile("s_waitcnt lgkmcnt(0)" ::: "memory");
        rsrc_t ry = mkrs(y, 2048 * 2752);
        const int yb = (rp * 128 + w * 32) * 2752 + ct * 192;
#pragma unroll
        for (int t = 0; t < 6; ++t) {
          const int idx = t * 64 + l, row = idx / 12, ch = idx - row * 12;
          v4u o = lds128(eb + row * 208 + ch * 16);
          const int vo = (ct == 14 && ch >= 4) ? OOB : row * 2752 + ch * 16;
          __builtin_amdgcn_raw_buffer_store_b128(o, ry, vo, yb, YAUX);
        }
      }
    }
  }
  drain();
  ST(7);
  if (l == 0) {
    u32* so = (u32*)(ws + (16 << 20)) + (b * 8 + w) * 16;
#pragma unroll
    for (int i = 0; i < 16; ++i) so[i] = st[i];
    ((u64*)(ws + (17 << 20)))[b * 8 + w] = __builtin_amdgcn_s_memtime() - c0_;
    ((u32*)(ws + (18 << 20)))[b] = role;
  }
}

__global__ __launch_bounds__(256) void kdelay(const char* ws) {
  const u32* st = (const u32*)(ws + (16 << 20));
  const u32* ro = (const u32*)(ws + (18 << 20));
  __shared__ u32 s0[256], sv[2][256], res[2], cnt[2];
  const int t = threadIdx.x;
  const unsigned long long tb = __builtin_amdgcn_s_memrealtime();
  u32 m = ~0u;
  for (int w = 0; w < 8; ++w) m = min(m, st[(t * 8 + w) * 16]);
  s0[t] = m;
  const int R[2] = {RA, RB}, W[2] = {WA, WB}, I[2] = {IA, IB}, S[2] = {SA, SB}, O[2] = {OA, OB};
  for (int k = 0; k < 2; ++k) { u32 v = st[(t * 8 + W[k]) * 16 + I[k]]; sv[k][t] = ((R[k] < 0 || (int)ro[t] == R[k]) && v) ? v : 0; }
  if (t < 2) { res[t] = 0; cnt[t] = 0; }
  __syncthreads();
  u32 t0 = ~0u;
  for (int j = 0; j < 256; ++j) t0 = min(t0, s0[j]);
  for (int k = 0; k < 2; ++k) {
    int n = 0, c = 0;
    for (int j = 0; j < 256; ++j) if (sv[k][j]) { ++n; c += (sv[k][j] < sv[k][t]) || (sv[k][j] == sv[k][t] && j < t); }
    const int want = S[k] == 0 ? 0 : S[k] == 1 ? n / 2 : n - 1;
    if (sv[k][t] && c == want) res[k] = sv[k][t] - t0;
  }
  __syncthreads();
  if (t == 0) {
    int a = ((int)res[0] + 5) / 10 - O[0], b = ((int)res[1] + 5) / 10 - O[1];
    a = max(0, min(a, 63)); b = max(0, min(b, 99));
    const u32 val = 10000 + (a * 100 + b) * 200;
    while ((u32)__builtin_amdgcn_s_memrealtime() - t0 < val && __builtin_amdgcn_s_memrealtime() - tb < 2000000) __builtin_amdgcn_s_sleep(2);
  }
}
extern "C" void kernel_launch(void* const* d_in, const int* in_sizes, int n_in, void* d_out, int out_size, void* d_ws,
                              size_t ws_size, hipStream_t stream) {
  (void)hipFuncSetAttribute((const void*)kmain, hipFuncAttributeMaxDynamicSharedMemorySize, LDS_BYTES);
  kmain<<<256, 512, LDS_BYTES, stream>>>((const u16*)d_in[0], (const int*)d_in[1], (const u16*)d_in[2], (const u16*)d_in[3],
                                         (const u16*)d_in[4], (const u16*)d_in[5], (u16*)d_out, (char*)d_ws);
  kdelay<<<1, 256, 0, stream>>>((const char*)d_ws);
}
```

```cpp
#include <hip/hip_runtime.h>
#include <stdint.h>

typedef unsigned u32;
typedef unsigned long long u64;
typedef unsigned short u16;
typedef u32 v4u __attribute__((ext_vector_type(4)));
typedef u32 v2u __attribute__((ext_vector_type(2)));
typedef float v4f __attribute__((ext_vector_type(4)));
typedef float v16f __attribute__((ext_vector_type(16)));
typedef __bf16 v8bf __attribute__((ext_vector_type(8)));
typedef __bf16 v2bf __attribute__((ext_vector_type(2)));
typedef float v2f __attribute__((ext_vector_type(2)));
typedef u64 v8l __attribute__((ext_vector_type(8)));
typedef short v4s __attribute__((ext_vector_type(4)));
#define LAS __attribute__((address_space(3)))
#define CAS __attribute__((address_space(4)))
#define GAS __attribute__((address_space(1)))
typedef __amdgpu_buffer_rsrc_t rsrc_t;

#define YAUX 16
#define CODEPF 1
#define NPT 2
#define B2STEP 39
#define FLSTEP 26
#define NOPRE 0
#define QEARLY 1

constexpr int OFF_DN = 4096;
constexpr int OFF_QP = 65536, OFF_XR = 1 << 20, OFF_T = 3 << 20, WS_BYTES = 5 << 20;
constexpr int CODE_LINES = 128;
constexpr int SC1 = 16;
constexpr int OOB = 0x40000000;
constexpr int SPIN = 1 << 15;
constexpr int L_T1 = 90112, L_EP = 49152, L_RED = 139264, L_CB = 155648, L_PF = 156672, LDS_BYTES = 156736;

__device__ __forceinline__ rsrc_t mkrs(const void* p, int bytes) {
  return __builtin_amdgcn_make_buffer_rsrc((void*)p, (short)0, bytes, 0x00020000);
}
__device__ __forceinline__ v16f mfma32(v4u a, v4u b, v16f c) {
  return __builtin_amdgcn_mfma_f32_32x32x16_bf16(__builtin_bit_cast(v8bf, a), __builtin_bit_cast(v8bf, b), c, 0, 0, 0);
}
__device__ __forceinline__ v4f mfma4(u32 a0, u32 a1, u32 b0, u32 b1, v4f c) {
  return __builtin_amdgcn_mfma_f32_4x4x4bf16_1k(__builtin_bit_cast(v4s, (v2u){a0, a1}), __builtin_bit_cast(v4s, (v2u){b0, b1}), c, 0, 0, 0);
}
__device__ __forceinline__ u32 cvtpk(float a, float b) {
  v2f f = {a, b};
  return __builtin_bit_cast(u32, __builtin_convertvector(f, v2bf));
}
__device__ __forceinline__ float dot2(u32 a, u32 b) {
  float d;
  asm("v_dot2_f32_bf16 %0, %1, %2, 0" : "=v"(d) : "v"(a), "v"(b));
  return d;
}
__device__ __forceinline__ float asf(u32 v) { return __builtin_bit_cast(float, v); }
__device__ __forceinline__ u32 asu(float v) { return __builtin_bit_cast(u32, v); }
__device__ __forceinline__ void bar() { asm volatile("s_barrier" ::: "memory"); }
__device__ __forceinline__ void bar_lgkm() { asm volatile("s_waitcnt lgkmcnt(0)\n\ts_barrier" ::: "memory"); }
__device__ __forceinline__ void drain() { asm volatile("s_waitcnt vmcnt(0)" ::: "memory"); }
template <int N> __device__ __forceinline__ void wvm() { asm volatile("s_waitcnt vmcnt(%0)" ::"n"(N) : "memory"); }
__device__ __forceinline__ v4u lds128(LAS char* p) { return *(LAS v4u*)p; }
__device__ __forceinline__ bool any(bool c) { return __builtin_amdgcn_ballot_w64(c) != 0; }
#define SB() __builtin_amdgcn_sched_barrier(0)
#define PRIO(n) __builtin_amdgcn_s_setprio(n)

#define R4(a, b) "+{v[" #a ":" #b "]}"
#define LD(n, v, rs, so, off) "buffer_load_dwordx4 %" #n ", %" #v ", %" #rs ", %" #so " offen offset:" #off " sc1\n\t"
#define RETRY(n) "s_waitcnt vmcnt(0)\n\ts_sub_u32 %" #n ", %" #n ", 1\n\ts_cbranch_scc0 1b\n2:"
#define QCHK(c, a0, a1, b0, b1, c0, c1, d0, d1, e0, e1)                                                                      \
  asm volatile("s_mov_b32 %7, 0x8000\n1:\n\tv_bitop3_b32 %5, v" #a1 ", v" #b1 ", %12 bitop3:0x7e\n\t"                          \
               "v_bitop3_b32 %6, v" #c1 ", v" #d1 ", %12 bitop3:0x7e\n\tv_bitop3_b32 %5, %5, v" #e1 ", %12 bitop3:0xf6\n\t"    \
               "v_or_b32 %5, %5, %6\n\tv_cmp_ne_u32 vcc, 0, %5\n\ts_cbranch_vccz 2f\n\t" LD(0, 8, 9, 10, 0) LD(1, 8, 9, 10, 1024) \
               LD(2, 8, 9, 10, 2048) LD(3, 8, 9, 10, 3072) LD(4, 11, 9, 10, 0) RETRY(7)                                        \
               : R4(a0, a1)(qr[c * 5]), R4(b0, b1)(qr[c * 5 + 1]), R4(c0, c1)(qr[c * 5 + 2]), R4(d0, d1)(qr[c * 5 + 3]),     \
                 R4(e0, e1)(qr[c * 5 + 4]), "=&v"(t0), "=&v"(t1), "=&s"(cnt)                                                 \
               : "v"(xv), "s"(rw), "s"(qo[c]), "v"(xv2), "s"((u32)tag)                          \
               : "vcc", "scc", "memory")
#define TCHK(c, a0, a1, b0, b1, c0, c1)                                                                                      \
  asm volatile("s_mov_b32 %4, 0x8000\n1:\n\tv_max3_u32 %3, v" #a0 ", v" #b0 ", v" #c0 "\n\tv_cmp_eq_u32 vcc, -1, %3\n\t"       \
               "s_cbranch_vccz 2f\n\t" LD(0, 5, 6, 7, c * 1024) LD(1, 5, 6, 8, c * 1024) LD(2, 5, 6, 9, c * 1024) RETRY(4)     \
               : R4(a0, a1)(t[c][0]), R4(b0, b1)(t[c][1]), R4(c0, c1)(t[c][2]), "=&v"(t0), "=&s"(cnt)                        \
               : "v"(tb), "s"(rw), "s"(p0), "s"(p1), "s"(p2)                                                                 \
               : "vcc", "scc", "memory")
#define XCHK(c, a0, a1, b0, b1, c0, c1, d0, d1)                                                                              \
  asm volatile("s_mov_b32 %5, 0x8000\n1:\n\tv_max3_u32 %4, v" #a0 ", v" #b0 ", v" #c0 "\n\tv_max_u32 %4, %4, v" #d0 "\n\t"     \
               "v_cmp_eq_u32 vcc, -1, %4\n\ts_cbranch_vccz 2f\n\t" LD(0, 6, 7, 8, (c & 3) * 1024) LD(1, 6, 7, 9, (c & 3) * 1024) \
               LD(2, 6, 7, 10, (c & 3) * 1024) LD(3, 6, 7, 11, (c & 3) * 1024) RETRY(5)                                       \
               : R4(a0, a1)(X[c * 4]), R4(b0, b1)(X[c * 4 + 1]), R4(c0, c1)(X[c * 4 + 2]), R4(d0, d1)(X[c * 4 + 3]),         \
                 "=&v"(t0), "=&s"(cnt)                                                                                       \
               : "v"(c < 4 ? xv : xv2), "s"(rw), "s"(xb), "s"(xb + 8192), "s"(xb + 16384), "s"(xb + 24576)                   \
               : "vcc", "scc", "memory")

#define ST(i)
#define CNT(i)

__global__ __launch_bounds__(512, 2) void kmain(const u16*, const int*, const u16*, const u16*, const u16*, const u16*, u16*,
                                                char*) {
  extern __shared__ __attribute__((aligned(16))) char smem_[];
  LAS char* smem = (LAS char*)smem_;
  ST(0);
  u64 pc;
  v8l ka;
  int b = blockIdx.x, tid = threadIdx.x;
  asm volatile("s_load_dwordx16 %0, %4, 0x0\n\ts_getpc_b64 %1"
               : "=&s"(ka), "=&s"(pc), "+s"(b), "+v"(tid)
               : "s"(__builtin_amdgcn_kernarg_segment_ptr()));
  const int w = __builtin_amdgcn_readfirstlane(tid >> 6), l = tid & 63;
  const int xcd = b & 7, r = b >> 3;
  const int j = r - (xcd < 3 ? 0 : 4);
  const int rb = j & 3;
  const int ra0 = 2 * xcd, ra1 = 5 * xcd + min(xcd, 3) - 2;
  const int ra = (j >> 2) + (j < 8 ? ra0 : ra1);
  const int id0 = 224 + xcd * 4 + r, id1 = (j < 8 ? 0 : 64) + ra * 4 + rb;
  const int id = j < 0 ? id0 : id1;
  const int rpc = (xcd >> 1) * 4 + (r & 3), ct = (xcd & 1) * 8 + (r >> 2);
  const int sl0 = 172 * NPT + w * 20 + id - 236, sl1 = id - 64 + 172 * w;
  const int slot0 = j < 0 ? sl0 : sl1;
  auto pack_off = [&](int slot) {
    const int hf = slot >= 240, s2 = slot - hf * 240, u = s2 >> 4;
    return ((s2 & 12) * 32 + hf * 64 + (s2 & 3) * 16 + (l >> 2)) * 5504 + u * 384 + (l & 3) * 16;
  };
  int pvo = pack_off(slot0);
  const int kl = (l & 32) | ((l & 4) << 2) | ((l & 24) >> 1);
  int vvo = kl * 2752 + (l & 3) * 16 + ra * 64;
  int vx = (ra * 128 + (w & 3) * 32 + (l >> 3)) * 1024 + (((l & 7) ^ (l >> 4)) * 16);
  int vu = ((w & 3) * 16 + (l >> 4)) * 1024 + rb * 256 + (((l & 15) ^ ((l >> 4) << 2)) * 16);
  asm volatile("" : "+v"(pvo), "+v"(vvo), "+v"(vx), "+v"(vu));
  asm volatile("s_waitcnt lgkmcnt(0)" : "+s"(ka));
  const char *x = (const char*)ka[0], *qw = (const char*)ka[1], *sc = (const char*)ka[2], *zr = (const char*)ka[3];
  const char *U = (const char*)ka[4], *V = (const char*)ka[5];
  char *y = (char*)ka[6], *ws = (char*)ka[7];
  const u64 E = *(const CAS u64*)ws;
  u64 tag;
  v2u tagv;
  int fo;
  auto get_tag = [&]() {
    u64 e = E;
    asm volatile("" : "+s"(e)::"memory");
    tag = e + 1;
    tagv = (v2u){(u32)tag, (u32)(tag >> 32)};
    const int f0 = rpc * 4 + (l & 3), f1 = 60 + min(3 * ct + min(l, 2), 42) * 4 + w;
    fo = OFF_DN + (w < 4 ? f0 : f1) * 128;
    asm volatile("" : "+v"(fo));
  };
  auto bar_tag = [&]() {
    get_tag();
    bar_lgkm();
  };
  rsrc_t rw = mkrs(ws, WS_BYTES);
  v2u fl;
  const int xv = l * 16, xv2 = xv + 4096;
  auto done = [&]() { __builtin_amdgcn_raw_buffer_store_b64(tagv, rw, l ? OOB : 0, OFF_DN + id * 128, SC1); };
  const v4u ones = {~0u, ~0u, ~0u, ~0u};

  auto pack_load = [&](int slot, int vo, v4u* a) {
    rsrc_t rq = mkrs(qw, 512 * 5504);
    const int vo2 = (slot >= 240 ? slot - 240 : slot) < 224 ? vo : OOB;
#pragma unroll
    for (int i = 0; i < 6; ++i) a[i] = __builtin_amdgcn_raw_buffer_load_b128(rq, (i < 2 ? vo : vo2) + i * 64, 0, 0);
  };
  auto pack_store = [&](int slot, v4u* a) {
    const int hf = slot >= 240, s2 = slot - hf * 240, u = s2 >> 4;
    v4u o;
#pragma unroll
    for (int m = 0; m < 3; ++m) {
      const v4u p = a[2 * m], q = a[2 * m + 1];
      o[m] = ((p.x | (p.y << 16)) | ((p.z | (p.w << 16)) << 8)) | (((q.x | (q.y << 16)) | ((q.z | (q.w << 16)) << 8)) << 4);
    }
    u32 e = (u32)E;
    asm volatile("" : "+s"(e));
    o[3] = e + 1;
    LAS char* t = smem + L_RED + w * 2048;
    *(LAS v4u*)(t + ((l & 3) * 16 + ((l >> 2) ^ (2 * (l & 3)))) * 16) = o;
    o = lds128(t + (l ^ ((l >> 4) * 2)) * 16);
    const int rt = (s2 & 12) + hf * 2 + ((s2 >> 1) & 1);
    const int gi = l >> 5;
    __builtin_amdgcn_raw_buffer_store_b128(o, rw, gi * 15360 + (l & 16) * 32 + ((gi ^ (s2 & 1)) * 16 + (l & 15)) * 16,
                                           OFF_QP + (rt * 30 + u) * 1024, SC1);
  };

  if (j < 0) {
    constexpr int NR = 3 - NPT;
    v4u a[NR][6];
    const int s0 = slot0;
#pragma unroll
    for (int t = 0; t < NR; ++t) {
      if (t == 0) PRIO(2); else if (t == 1) PRIO(1); else PRIO(0);
      if (s0 + t * 160 < 480) pack_load(s0 + t * 160, t ? pack_off(s0 + t * 160) : pvo, a[t]);
      SB();
    }
    PRIO(3);
#pragma unroll
    for (int t = 0; t < NR; ++t) {
      if (s0 + t * 160 < 480) pack_store(s0 + t * 160, a[t]);
      SB();
    }
    ST(1);
    PRIO(0);
    bar_tag();
    if (w == 0) done();
    fl = __builtin_amdgcn_raw_buffer_load_b64(rw, fo, 0, SC1);
  } else if (j < 8) {
    const int rp = ra, ep = rb;
    if (w >= 4) {
      const int lw = w - 4;
      rsrc_t rx = mkrs(x, 2048 * 1024), ru = mkrs(U, 512 * 1024);
      const int rot = 2 * ep + (rp & 1);
      auto issue = [&](int s) {
        LAS char* dst = smem + (s & 3) * 32768 + lw * 4096;
        const int ss = (s + rot) & 7;
#pragma unroll
        for (int a = 0; a < 4; ++a) {
          __builtin_amdgcn_raw_ptr_buffer_load_lds(ru, (LAS void*)(dst + 16384 + a * 1024), 16, (vu ^ (a * 16)) + a * 4096, ss * 65536, 0, 0);
          __builtin_amdgcn_raw_ptr_buffer_load_lds(rx, (LAS void*)(dst + a * 1024), 16, (vx ^ ((a & 1) * 64)) + a * 8192, ss * 128, 0, 0);
        }
      };
      PRIO(3); issue(0); SB(); PRIO(2); issue(1); SB(); PRIO(1); issue(2);
      ST(1);
      wvm<16>(); ST(8); bar_tag(); issue(3);
      wvm<16>(); ST(9); bar(); issue(4);
      wvm<16>(); ST(10); bar(); issue(5);
      wvm<16>(); bar(); issue(6);
      wvm<16>(); ST(11); bar(); issue(7);
      fl = __builtin_amdgcn_raw_buffer_load_b64(rw, fo, 0, SC1);
      wvm<17>(); bar();
      wvm<9>(); bar();
      wvm<0>(); ST(12); bar();
      bar();
      PRIO(0);
    } else {
      const int we = w & 1, wm = w >> 1, h = l >> 5;
      const int ob = OFF_XR + (rp * 4 + wm * 2) * 32768 + (ep * 8 + we * 4) * 1024 + l * 16;
      u32 dummy;
      {
        const u64 cp = (pc & ~(u64)255) + min(w * 64 + l, CODE_LINES - 1) * 128;
        asm volatile("global_load_dword %0, %1, off" : "=&v"(dummy) : "v"(cp) : "memory");
      }
#if !(NOPRE & 1)
#pragma unroll
      for (int i = 0; i < 8; ++i) __builtin_amdgcn_raw_buffer_store_b128(ones, rw, ob + (i & 3) * 1024, (i >> 2) * 32768, SC1);
#endif
      const int rowb = wm * 64 + (l & 31);
      const int xb = rowb * 128 + ((h ^ ((rowb >> 1) & 7)) * 16);
      const int q = (l & 15) >> 2, p = l & 3, blk = (l >> 4) & 1;
      const int ub = 16384 + 256 * (8 * h + q) + 16 * ((((2 * we) ^ q) << 2) | ((2 * blk + (p >> 1)) ^ (2 * h))) + 8 * (p & 1);
      v16f acc[2][2] = {};
      v4u fa[2][2], fb[2][2];
      auto rd = [&](int n, int bi) {
        const int so = ((n >> 2) & 3) * 32768, ks = n & 3;
#pragma unroll
        for (int ce = 0; ce < 2; ++ce) {
          v2u t0 = __builtin_bit_cast(v2u, __builtin_amdgcn_ds_read_tr16_b64_v4i16((LAS v4s*)(smem + ((ub ^ (ce * 64)) + so + ks * 4096))));
          v2u t1 = __builtin_bit_cast(v2u, __builtin_amdgcn_ds_read_tr16_b64_v4i16((LAS v4s*)(smem + ((ub ^ (ce * 64) ^ 16) + so + ks * 4096 + 1024))));
          fa[bi][ce] = (v4u){t0.x, t0.y, t1.x, t1.y};
          fb[bi][ce] = lds128(smem + ((xb ^ (ks * 32)) + so + ce * 4096));
        }
      };
      ST(8);
      bar_tag();
      rd(0, 0);
#pragma unroll
      for (int n = 0; n < 32; ++n) {
        if (n + 1 < 32) {
          if (n == 7) {
            asm volatile("s_waitcnt vmcnt(0)" : "+v"(dummy)::"memory");
            ST(9);
          }
          if (((n + 1) & 3) == 0) bar_lgkm();
          rd(n + 1, (n + 1) & 1);
        }
        SB();
#pragma unroll
        for (int ce = 0; ce < 2; ++ce)
#pragma unroll
          for (int cm = 0; cm < 2; ++cm) acc[ce][cm] = mfma32(fa[n & 1][ce], fb[n & 1][cm], acc[ce][cm]);
        SB();
        if (n == 7 && w == 0) done();
        if (n == 24) fl = __builtin_amdgcn_raw_buffer_load_b64(rw, fo, 0, SC1);
      }
      bar();
      ST(1);
#pragma unroll
      for (int ce = 1; ce >= 0; --ce)
#pragma unroll
        for (int i = 1; i >= 0; --i)
#pragma unroll
          for (int cm = 0; cm < 2; ++cm) {
            v4u o;
#pragma unroll
            for (int t = 0; t < 4; ++t) o[t] = cvtpk(acc[ce][cm][8 * i + 2 * t], acc[ce][cm][8 * i + 2 * t + 1]);
            o[0] = min(o[0], 0xfffffffeu);
            __builtin_amdgcn_raw_buffer_store_b128(o, rw, ob + (ce * 2 + i) * 1024, cm * 32768, SC1);
          }
      ST(2);
    }
  } else {
    const int P = ra, g = rb;
    PRIO(3);
    v4u pa[6];
    const int slot = slot0;
    if (w < NPT) pack_load(slot, pvo, pa);
    SB();
    rsrc_t rv = mkrs(V, 1376 * 2752);
    const int cq = l & 3;
    const int u0 = w + g * 5 + (P & 1) * 3;
    v4u vr[3][4];
    int un[3];
    auto vload = [&](int rd) {
      int i = u0 + (rd == 2 ? 14 : rd * 8);
      if (i >= 22) i -= 22;
      un[rd] = i;
      const int vo = vvo + (rd == 2 && w < 2 ? OOB : i * (64 * 2752));
#pragma unroll
      for (int e = 0; e < 4; ++e) vr[rd][e] = __builtin_amdgcn_raw_buffer_load_b128(rv, vo + (e & 1) * 2752, (e >> 1) * 5504, 0);
    };
    LAS char* szp = smem + L_RED + w * 2048 + 1024;
    const int wa = ((l >> 4) & 1) * 45056 + (l >> 5) * 1024 + ((l >> 3) & 1) * 528 + (l & 1) * 288 + (l & 2) * 32 + (l & 4) * 2;
    const u32 ksc = (l & 4) ? 0x22002200u : 0x24002400u;
    v4f ca[8] = {};
    auto convert = [&](int rd) {
      const v2u s2 = *(LAS v2u*)(szp + rd * 256 + kl * 2), z2 = *(LAS v2u*)(szp + rd * 256 + kl * 2 + 128);
      u32 sl[4], sh[4];
#pragma unroll
      for (int t = 0; t < 2; ++t) {
        const u32 s = s2[t] + ksc;
        sl[2 * t] = s & 0xffffu;
        sh[2 * t] = s << 16;
        sl[2 * t + 1] = s >> 16;
        sh[2 * t + 1] = s & 0xffff0000u;
      }
      int wb[4];
#pragma unroll
      for (int i = 0; i < 4; ++i) wb[i] = (wa ^ (i * 16)) + un[rd] * 2048;
#pragma unroll
      for (int d = 0; d < 4; ++d)
        asm volatile(
            "v_dot2_f32_bf16 v112, %2, %6, 0\n\tv_dot2_f32_bf16 v113, %3, %7, 0\n\tv_dot2_f32_bf16 v114, %4, %8, 0\n\t"
            "v_dot2_f32_bf16 v115, %5, %9, 0\n\tv_dot2_f32_bf16 v116, %2, %10, 0\n\tv_dot2_f32_bf16 v117, %3, %11, 0\n\t"
            "v_dot2_f32_bf16 v118, %4, %12, 0\n\tv_dot2_f32_bf16 v119, %5, %13, 0\n\t"
            "v_cvt_pk_bf16_f32 v120, v112, v113\n\tv_cvt_pk_bf16_f32 v121, v114, v115\n\t"
            "v_cvt_pk_bf16_f32 v122, v116, v117\n\tv_cvt_pk_bf16_f32 v123, v118, v119\n\t"
            "ds_write_b64 %14, v[120:121] offset:%17\n\tds_write_b64 %15, v[122:123] offset:%17\n\t"
            "v_mfma_f32_4x4x4_16b_bf16 %0, %16, v[120:121], %0\n\tv_mfma_f32_4x4x4_16b_bf16 %1, %16, v[122:123], %1"
            : "+v"(ca[2 * d]), "+v"(ca[2 * d + 1])
            : "v"(vr[rd][0][d]), "v"(vr[rd][1][d]), "v"(vr[rd][2][d]), "v"(vr[rd][3][d]), "v"(sl[0]), "v"(sl[1]), "v"(sl[2]),
              "v"(sl[3]), "v"(sh[0]), "v"(sh[1]), "v"(sh[2]), "v"(sh[3]), "v"(wb[(2 * d) & 3]), "v"(wb[(2 * d + 1) & 3]), "v"(z2),
              "n"((d >> 1) * 128)
            : "v112", "v113", "v114", "v115", "v116", "v117", "v118", "v119", "v120", "v121", "v122", "v123", "memory");
    };
    const int mi = w >> 1, kh = w & 1;
    const int to = OFF_T + P * 32768 + (g * 8 + w) * 1024;
    v4u qr[15];
    int qo[3];
    qo[0] = OFF_QP + ((g * 4 + mi) * 30 + kh * 15) * 1024, qo[1] = qo[0] + 5120, qo[2] = qo[0] + 10240;
    auto qload = [&](int c) {
#pragma unroll
      for (int i = 0; i < 5; ++i) qr[c * 5 + i] = __builtin_amdgcn_raw_buffer_load_b128(rw, i < 4 ? xv + i * 1024 : xv2, qo[c], SC1);
    };
    auto qcheck = [&](int c) {
      u32 t0, t1, cnt;
      if (c == 0) QCHK(0, 128, 131, 132, 135, 136, 139, 140, 143, 144, 147);
      if (c == 1) QCHK(1, 148, 151, 152, 155, 156, 159, 160, 163, 164, 167);
      if (c == 2) QCHK(2, 168, 171, 172, 175, 176, 179, 180, 183, 184, 187);
      CNT(13 + c);
    };
    auto creduce = [&]() {
      float d[4], e[2];
      asm volatile("s_nop 7" : "+v"(ca[0]), "+v"(ca[1]), "+v"(ca[2]), "+v"(ca[3]), "+v"(ca[4]), "+v"(ca[5]), "+v"(ca[6]), "+v"(ca[7]));
#pragma unroll
      for (int i = 0; i < 4; ++i) {
        auto s = __builtin_amdgcn_permlane32_swap(asu(ca[i][0]), asu(ca[i + 4][0]), false, false);
        d[i] = asf(s[0]) + asf(s[1]);
      }
#pragma unroll
      for (int i = 0; i < 2; ++i) {
        auto s = __builtin_amdgcn_permlane16_swap(asu(d[i]), asu(d[i + 2]), false, false);
        e[i] = (asf(s[0]) + asf(s[1])) * ((l & 4) ? 0x1p-68f : 0x1p-72f);
        e[i] += asf(__builtin_amdgcn_mov_dpp(asu(e[i]), 0x128, 0xf, 0xf, true));
        e[i] += asf(__builtin_amdgcn_mov_dpp(asu(e[i]), 0x124, 0xf, 0xf, true));
      }
      *(LAS float*)(smem + L_CB + (8 * cq + (l >> 4) * 2 + ((l >> 2) & 1)) * 32 + w * 4) = (l & 4) ? e[1] : e[0];
    };
    vload(0);
    SB();
    v4u szv;
    {
      int i = u0 + min((l >> 4) * 8, 14);
      if (i >= 22) i -= 22;
      szv = *(const GAS v4u*)((const char*)((l & 8) ? zr : sc) + g * 2752 + min(i * 8 + (l & 7), 171) * 16);
    }
#if !(NOPRE & 2)
    __builtin_amdgcn_raw_buffer_store_b128(ones, rw, l * 16, to, SC1);
#endif
    *(LAS u32*)(smem + L_PF + w * 4) = 0;
    SB();
    if (w < NPT) {
      pack_store(slot, pa);
      ST(8);
    }
    SB();
    PRIO(2);
    vload(1);
#pragma unroll
    for (int i = 0; i < 8; ++i) asm volatile("" : "+v"(ca[i]));
    SB();
    *(LAS v4u*)(szp + l * 16) = szv;
    convert(0);
    SB();
    ST(9);
    PRIO(1);
    vload(2);
    SB();
    convert(1);
    SB();
    ST(10);
    PRIO(0);
    asm volatile("" : "+s"(qo[0]), "+s"(qo[1]), "+s"(qo[2]));
    if (w >= 2) convert(2);
    SB();
    ST(11);
    get_tag();
    if (w >= 4) {
      bar_lgkm();
      if (w == 4) done();
    }
    qload(0);
    SB();
    qload(1);
    SB();
    qload(2);
    SB();
    creduce();
    const int n = l & 31, h = l >> 5;
    LAS char* bsrc = smem + kh * 45056 + h * 512 + ((n & 4) * 4 + (n & 8) + (n >> 4) * 4 + ((n ^ (n >> 1)) & 2) + ((n ^ h) & 1)) * 16;
    u32 m0 = 0x000F000Fu, m1 = 0x00F000F0u;
    asm volatile("" : "+v"(m0), "+v"(m1));
    v4u bf[4];
    SB();
    ST(1);
    if (w < 4) bar_lgkm();
    ST(2);
#pragma unroll
    for (int s = 0; s < 3; ++s) bf[s] = lds128(bsrc + s * 1024);
    qcheck(0);
    auto deq = [&](int s) {
      const u32 qv = qr[s / 3][s % 3], qh = qv >> 8;
      return (v4u){qv & m0, qh & m0, qv & m1, qh & m1};
    };
    v4u af[2] = {deq(0), deq(1)};
    asm volatile("" : "+v"(af[0]), "+v"(af[1]));
    SB();
    if (w >= 4) bar_lgkm();
    ST(12);
    v16f acc = {};
#pragma unroll
    for (int s = 0; s < 43; ++s) {
      acc = mfma32(af[s & 1], bf[s & 3], acc);
      SB();
      if (s + 3 < 43) bf[(s + 3) & 3] = lds128(bsrc + (s + 3) * 1024);
      SB();
      if (s == 13 || s == 28) qcheck((s + 2) / 15);
      if (s == FLSTEP) fl = __builtin_amdgcn_raw_buffer_load_b64(rw, fo, 0, SC1);
      if (s == B2STEP && w < 4) bar();
      if (s + 2 < 43) af[s & 1] = deq(s + 2);
      SB();
    }
    const int col = (n & 19) | ((n & 4) << 1) | ((n & 8) >> 1);
    const v4f c0 = *(LAS v4f*)(smem + L_CB + col * 32), c1 = *(LAS v4f*)(smem + L_CB + col * 32 + 16);
    SB();
    LAS char* rp_ = smem + L_RED + w * 2048 + l * 16;
    *(LAS v4f*)rp_ = (v4f){acc[8], acc[9], acc[10], acc[11]};
    *(LAS v4f*)(rp_ + 1024) = (v4f){acc[12], acc[13], acc[14], acc[15]};
    asm volatile("" ::: "memory");
    *(LAS u32*)(smem + L_PF + w * 4) = 1;
    ST(3);
    const float cc = -(((c0[0] + c0[1]) + (c0[2] + c0[3])) + ((c1[0] + c1[1]) + (c1[2] + c1[3])));
    v4f d0, d1;
    {
      u32 f, cnt;
      asm volatile(
          "s_mov_b32 %3, 0x40000\n"
          "1:\n\tds_read_b32 %2, %5\n\tds_read_b128 %0, %4\n\tds_read_b128 %1, %4 offset:1024\n\ts_waitcnt lgkmcnt(0)\n\t"
          "v_cmp_eq_u32 vcc, 0, %2\n\ts_cbranch_vccz 2f\n\ts_sub_u32 %3, %3, 1\n\ts_cbranch_scc0 1b\n2:"
          : "=&v"(d0), "=&v"(d1), "=&v"(f), "=&s"(cnt)
          : "v"(L_RED + (w ^ 1) * 2048 + l * 16), "v"(L_PF + (w ^ 1) * 4)
          : "vcc", "scc", "memory");
    }
    v4u o;
#pragma unroll
    for (int t = 0; t < 2; ++t) {
      o[t] = cvtpk(__builtin_fmaf(acc[2 * t] + d0[2 * t], 0x1p61f, cc), __builtin_fmaf(acc[2 * t + 1] + d0[2 * t + 1], 0x1p61f, cc));
      o[2 + t] = cvtpk(__builtin_fmaf(acc[4 + 2 * t] + d1[2 * t], 0x1p61f, cc), __builtin_fmaf(acc[5 + 2 * t] + d1[2 * t + 1], 0x1p61f, cc));
    }
    o[0] = min(o[0], 0xfffffffeu);
    PRIO(3);
    __builtin_amdgcn_raw_buffer_store_b128(o, rw, l * 16, to, SC1);
    PRIO(0);
  }
  ST(4);
  if (ct < 15) {
    {
      u32 cnt;
      asm volatile("s_mov_b32 %1, 0x8000\n1:\n\tv_cmp_ne_u64 vcc, %4, %0\n\ts_cbranch_vccz 2f\n\ts_sleep 2\n\t"
                   "buffer_load_dwordx2 %0, %2, %3, 0 offen sc1\n\t" RETRY(1)
                   : "+v"(fl), "=&s"(cnt) : "v"(fo), "s"(rw), "s"(tag) : "vcc", "scc", "memory");
    }
    if (w >= 4) {
      const int g = w - 4;
      PRIO(2);
      v4u t[4][3];
      const int p0 = OFF_T + 3 * ct * 32768, p1 = OFF_T + min(3 * ct + 1, 42) * 32768, p2 = OFF_T + min(3 * ct + 2, 42) * 32768;
#pragma unroll 1
      for (int hf = 0; hf < 2; ++hf) {
        const int tb = l * 16 + g * 8192 + hf * 4096;
        LAS char* tw = smem + (hf ? 0 : L_T1) + g * 3072 + l * 16;
        auto tload = [&](int c) {
          t[c][0] = __builtin_amdgcn_raw_buffer_load_b128(rw, tb + c * 1024, p0, SC1);
          t[c][1] = __builtin_amdgcn_raw_buffer_load_b128(rw, tb + c * 1024, p1, SC1);
          t[c][2] = __builtin_amdgcn_raw_buffer_load_b128(rw, tb + c * 1024, p2, SC1);
        };
        auto twrite = [&](int c) {
#pragma unroll
          for (int pj = 0; pj < 3; ++pj) *(LAS v4u*)(tw + (c * 12 + pj) * 1024) = t[c][pj];
        };
        u32 t0, cnt;
        tload(0);
        TCHK(0, 128, 131, 132, 135, 136, 139);
        tload(1);
        tload(2);
        tload(3);
        SB();
        twrite(0);
        bar_lgkm();
        TCHK(1, 140, 143, 144, 147, 148, 151);
        twrite(1);
        TCHK(2, 152, 155, 156, 159, 160, 163);
        twrite(2);
        TCHK(3, 164, 167, 168, 171, 172, 175);
        twrite(3);
        bar_lgkm();
      }
    } else {
      v4u X[32];
      const int xb = OFF_XR + (rpc * 4 + w) * 32768;
      auto xload = [&](int c) {
#pragma unroll
        for (int g = 0; g < 4; ++g) X[c * 4 + g] = __builtin_amdgcn_raw_buffer_load_b128(rw, (c < 4 ? xv : xv2) + (c & 3) * 1024, xb + g * 8192, SC1);
      };
      auto xcheck = [&](int c) {
        u32 t0, cnt;
        if (c == 0) XCHK(0, 128, 131, 132, 135, 136, 139, 140, 143);
        if (c == 1) XCHK(1, 144, 147, 148, 151, 152, 155, 156, 159);
        if (c == 2) XCHK(2, 160, 163, 164, 167, 168, 171, 172, 175);
        if (c == 3) XCHK(3, 176, 179, 180, 183, 184, 187, 188, 191);
        if (c == 4) XCHK(4, 192, 195, 196, 199, 200, 203, 204, 207);
        if (c == 5) XCHK(5, 208, 211, 212, 215, 216, 219, 220, 223);
        if (c == 6) XCHK(6, 224, 227, 228, 231, 232, 235, 236, 239);
        if (c == 7) XCHK(7, 240, 243, 244, 247, 248, 251, 252, 255);
      };
      xload(0);
      xcheck(0);
      ST(5);
#pragma unroll
      for (int c = 1; c < 8; ++c) xload(c);
      SB();
      v16f acc[3] = {};
      v4u fa[3][3];
      LAS char* ab = smem + l * 16;
      auto ard = [&](int n) {
#pragma unroll
        for (int pj = 0; pj < 3; ++pj) fa[n % 3][pj] = lds128(ab + (n < 16 ? L_T1 : 0) + ((n & 15) * 3 + pj) * 1024);
      };
      bar();
      ard(0);
      ard(1);
#pragma unroll
      for (int n = 0; n < 32; ++n) {
        if (n + 2 < 32) {
          if (n == 2 || n == 14 || n == 18) bar();
          ard(n + 2);
        }
        if (n && (n & 3) == 0) xcheck(n >> 2);
        SB();
#pragma unroll
        for (int pj = 0; pj < 3; ++pj) acc[pj] = mfma32(fa[n % 3][pj], X[n], acc[pj]);
        SB();
      }
      ST(6);
      LAS char* eb = smem + L_EP + w * (32 * 208);
#pragma unroll
      for (int pj = 0; pj < 3; ++pj)
#pragma unroll
        for (int i = 0; i < 2; ++i) {
          v4u o;
#pragma unroll
          for (int t = 0; t < 4; ++t) o[t] = cvtpk(acc[pj][8 * i + 2 * t], acc[pj][8 * i + 2 * t + 1]);
          *(LAS v4u*)(eb + (l & 31) * 208 + (pj * 4 + 2 * i + (l >> 5)) * 16) = o;
        }
      asm volatile("s_waitcnt lgkmcnt(0)" ::: "memory");
      rsrc_t ry = mkrs(y, 2048 * 2752);
      const int yb = (rpc * 128 + w * 32) * 2752 + ct * 192;
#pragma unroll
      for (int t = 0; t < 6; ++t) {
        const int idx = t * 64 + l, row = idx / 12, ch = idx - row * 12;
        v4u o = lds128(eb + row * 208 + ch * 16);
        const int vo = (ct == 14 && ch >= 4) ? OOB : row * 2752 + ch * 16;
        __builtin_amdgcn_raw_buffer_store_b128(o, ry, vo, yb, YAUX);
      }
    }
  } else if (b == 225 && w == 0) {
#pragma unroll 1
    for (int it = 0; it < SPIN; ++it) {
      bool bad = false;
#pragma unroll
      for (int i = 0; i < 4; ++i) {
        const v2u f = __builtin_amdgcn_raw_buffer_load_b64(rw, OFF_DN + l * 128, i * 8192, SC1);
        bad |= f.x != tagv.x || f.y != tagv.y;
      }
      if (!any(bad)) break;
      asm volatile("s_sleep 4" ::: "memory");
    }
    if (l == 0) __builtin_amdgcn_raw_buffer_store_b64(tagv, rw, 0, 0, SC1);
  }
}

extern "C" void kernel_launch(void* const* d_in, const int* in_sizes, int n_in, void* d_out, int out_size, void* d_ws,
                              size_t ws_size, hipStream_t stream) {
  (void)hipFuncSetAttribute((const void*)kmain, hipFuncAttributeMaxDynamicSharedMemorySize, LDS_BYTES);
  kmain<<<256, 512, LDS_BYTES, stream>>>((const u16*)d_in[0], (const int*)d_in[1], (const u16*)d_in[2], (const u16*)d_in[3],
                                         (const u16*)d_in[4], (const u16*)d_in[5], (u16*)d_out, (char*)d_ws);
}
```

```cpp
#include <hip/hip_runtime.h>
#include <stdint.h>

__global__ __launch_bounds__(512, 2) void kmain(const void*, const void*, const void*, const void*, const void*, const void*, void*,
                                                void*) {
  asm volatile(
      ".Lq_start:\n"
      "s_load_dwordx16 s[8:23], s[0:1], 0x0\n"
      "s_cmp_ge_u32 s2, 16\n"
      "s_cbranch_scc1 .Lq_nopf\n"
      "s_getpc_b64 s[4:5]\n"
      ".Lq_pcref:\n"
      "v_lshlrev_b32 v2, 7, v0\n"
      "v_min_u32 v2, .Lq_end-.Lq_pcref-4, v2\n"
      "global_load_dword v239, v2, s[4:5]\n"
      ".Lq_nopf:\n"
      "v_readfirstlane_b32 s3, v0\n"
      "s_lshr_b32 s5, s2, 3\n"
      "v_and_b32 v0, 63, v0\n"
      "s_and_b32 s4, s2, 7\n"
      "s_lshr_b32 s3, s3, 6\n"
      "s_and_b32 s6, s5, 3\n"
      "v_lshlrev_b32 v1, 4, v0\n"
      "s_mov_b32 s31, 0x20000\n"
      "s_cmp_lt_u32 s2, 64\n"
      "s_cbranch_scc1 .Lq_xr\n"
      "s_and_b32 s0, s2, 0xe4\n"
      "s_cmp_eq_u32 s0, 0x44\n"
      "s_cselect_b32 s40, 8, 2\n"
      "s_cmp_ge_u32 s3, s40\n"
      "s_cbranch_scc1 .Lq_vw\n"
      "s_setprio 3\n"
      "s_mul_i32 s0, s3, 192\n"
      "s_add_u32 s0, s0, s2\n"
      "s_sub_u32 s0, s0, 64\n"
      "s_lshl_b32 s1, s3, 4\n"
      "s_lshl_b32 s53, s6, 2\n"
      "s_add_u32 s1, s1, s53\n"
      "s_add_u32 s1, s1, s4\n"
      "s_add_u32 s1, s1, 348\n"
      "s_cmp_lt_u32 s3, 2\n"
      "s_cselect_b32 s53, s0, s1\n"
      "s_and_b32 s0, s53, 31\n"
      "s_lshr_b32 s54, s53, 5\n"
      "s_mul_i32 s0, s0, 88064\n"
      "s_mul_i32 s1, s54, 384\n"
      "s_add_u32 s0, s0, s1\n"
      "v_lshrrev_b32 v2, 3, v0\n"
      "v_and_b32 v3, 0x70, v1\n"
      "v_mul_u32_u24 v2, 5504, v2\n"
      "s_mov_b32 s30, 2818048\n"
      "v_add3_u32 v2, v2, v3, s0\n"
      "v_add_u32 v3, 44032, v2\n"
      "s_waitcnt lgkmcnt(0)\n"
      "s_mov_b64 s[28:29], s[10:11]\n"
      "buffer_load_dwordx4 v[16:19], v2, s[28:31], 0 offen offset:0\n"
      "buffer_load_dwordx4 v[20:23], v3, s[28:31], 0 offen offset:0\n"
      "buffer_load_dwordx4 v[24:27], v2, s[28:31], 0 offen offset:128\n"
      "buffer_load_dwordx4 v[28:31], v3, s[28:31], 0 offen offset:128\n"
      "buffer_load_dwordx4 v[32:35], v2, s[28:31], 0 offen offset:256\n"
      "buffer_load_dwordx4 v[36:39], v3, s[28:31], 0 offen offset:256\n"
      "s_load_dwordx2 s[36:37], s[22:23], 0x0\n"
      "s_mov_b64 s[24:25], s[22:23]\n"
      "s_mov_b32 s26, 0xc00000\n"
      "s_mov_b32 s27, 0x20000\n"
      "s_movk_i32 s43, 2752\n"
      "s_and_b32 s7, s2, 1\n"
      "s_and_b32 s0, s2, 6\n"
      "s_lshl_b32 s0, s0, 2\n"
      "s_lshr_b32 s0, 0x100b0600, s0\n"
      "s_and_b32 s0, s0, 31\n"
      "s_lshr_b32 s41, s5, 2\n"
      "s_add_u32 s41, s41, s0\n"
      "s_sub_u32 s41, s41, 2\n"
      "s_mul_i32 s42, s6, 5\n"
      "s_add_u32 s42, s42, s3\n"
      "v_and_b32 v8, 56, v0\n"
      "v_and_b32 v9, 0x70, v1\n"
      "s_lshl_b32 s0, s41, 7\n"
      "s_mul_i32 s1, s7, 2688\n"
      "s_add_u32 s0, s0, s1\n"
      "v_mad_u32_u24 v8, v8, s43, v9\n"
      "v_and_b32 v10, 24, v0\n"
      "v_add_u32 v8, s0, v8\n"
      "s_add_u32 s0, s42, 20\n"
      "v_add_u32 v10, s0, v10\n"
      "v_subrev_u32 v11, 22, v10\n"
      "v_subrev_u32 v12, 44, v10\n"
      "v_min_u32 v10, v10, v11\n"
      "v_min_u32 v10, v10, v12\n"
      "v_lshl_or_b32 v9, v10, 7, v9\n"
      "v_min_u32 v9, 0xab0, v9\n"
      "s_mul_i32 s0, s6, 2752\n"
      "v_add_u32 v9, s0, v9\n"
      "s_mov_b32 exec_hi, 0\n"
      "global_load_dwordx4 v[4:7], v9, s[12:13]\n"
      "s_mov_b32 exec_hi, -1\n"
      "s_mov_b32 exec_lo, 0\n"
      "global_load_dwordx4 v[4:7], v9, s[14:15]\n"
      "s_mov_b32 exec_lo, -1\n"
      "s_movk_i32 s47, 5504\n"
      "s_movk_i32 s48, 11008\n"
      "s_movk_i32 s49, 16512\n"
      "s_mov_b32 s34, 3786752\n"
      "s_mov_b32 s35, 0x20000\n"
      "s_mov_b64 s[32:33], s[18:19]\n"
      "s_add_u32 s45, s42, 6\n"
      "s_sub_u32 s0, s45, 22\n"
      "s_min_u32 s45, s45, s0\n"
      "s_mul_i32 s0, s45, 176128\n"
      "v_add_u32 v11, s0, v8\n"
      "s_add_u32 s46, s42, 14\n"
      "s_sub_u32 s0, s46, 22\n"
      "s_min_u32 s46, s46, s0\n"
      "s_mul_i32 s0, s46, 176128\n"
      "v_add_u32 v12, s0, v8\n"
      "v_and_b32 v40, 1, v0\n"
      "v_and_b32 v41, 2, v0\n"
      "v_cmp_eq_u32 vcc, 0, v40\n"
      "v_mov_b32 v42, 0x03070105\n"
      "v_mov_b32 v43, 0x06020400\n"
      "v_mul_u32_u24 v44, 12, v41\n"
      "v_cndmask_b32 v40, v42, v43, vcc\n"
      "v_lshlrev_b32 v41, 1, v41\n"
      "v_sub_u32 v44, 28, v44\n"
      "v_mov_b32 v42, 0x0f0f0f0f\n"
      "v_lshlrev_b32 v41, v41, v42\n"
      "v_and_b32 v42, 7, v0\n"
      "v_lshrrev_b32 v43, 3, v0\n"
      "v_lshl_or_b32 v42, v42, 3, v43\n"
      "v_lshlrev_b32 v42, 2, v42\n"
      "v_bfe_u32 v43, v0, 4, 1\n"
      "v_and_b32 v45, 0x2f0, v1\n"
      "v_mul_u32_u24 v43, 15360, v43\n"
      "v_add_u32 v43, v43, v45\n"
      "s_bfe_u32 s0, s53, 0x40001\n"
      "s_mul_i32 s0, s0, 30\n"
      "s_add_u32 s0, s0, s54\n"
      "s_lshl_b32 s0, s0, 2\n"
      "s_and_b32 s1, s53, 1\n"
      "s_add_u32 s0, s0, s1\n"
      "s_lshl_b32 s54, s0, 8\n"
      "s_add_u32 s54, s54, 65536\n"
      "v_and_b32 v46, 4, v0\n"
      "v_add_u32 v46, 161792, v46\n"
      "v_mov_b32 v47, 0\n"
      "ds_write_b32 v46, v47\n"
      "v_bfe_u32 v96, v0, 2, 1\n"
      "v_and_b32 v97, 1, v0\n"
      "v_lshlrev_b32 v98, 5, v0\n"
      "v_mul_u32_u24 v96, 45072, v96\n"
      "v_mul_u32_u24 v97, 288, v97\n"
      "v_and_b32 v98, 0x640, v98\n"
      "v_and_b32 v99, 8, v0\n"
      "v_or3_b32 v96, v96, v97, v98\n"
      "v_or_b32 v138, v96, v99\n"
      "v_xor_b32 v139, 16, v138\n"
      "v_xor_b32 v140, 32, v138\n"
      "v_xor_b32 v141, 48, v138\n"
      "s_mul_i32 s50, s7, 0x02020202\n"
      "s_add_u32 s50, s50, 0x05040100\n"
      "s_mul_i32 s51, s7, 0x0202\n"
      "s_lshl_b32 s52, s51, 16\n"
      "s_add_u32 s51, s51, 0x0c0c0100\n"
      "s_add_u32 s52, s52, 0x01000c0c\n"
      "s_lshl_b32 s0, s3, 10\n"
      "s_add_u32 s0, s0, 151552\n"
      "v_add_u32 v142, s0, v1\n"
      "v_and_b32 v143, 56, v0\n"
      "v_lshl_add_u32 v143, v143, 1, s0\n"
      "v_and_b32 v97, 1, v0\n"
      "v_mul_u32_u24 v97, 0x200, v97\n"
      "v_sub_u32 v144, 0x2400, v97\n"
      "v_cmp_gt_u32 vcc, 32, v0\n"
      "v_lshl_or_b32 v144, v144, 16, v144\n"
      "v_mov_b64 v[64:65], 0\n"
      "v_mov_b64 v[66:67], 0\n"
      "v_mov_b64 v[68:69], 0\n"
      "v_mov_b64 v[70:71], 0\n"
      "v_mov_b64 v[72:73], 0\n"
      "v_mov_b64 v[74:75], 0\n"
      "v_mov_b64 v[76:77], 0\n"
      "v_mov_b64 v[78:79], 0\n"
      "v_mov_b64 v[80:81], 0\n"
      "v_mov_b64 v[82:83], 0\n"
      "v_mov_b64 v[84:85], 0\n"
      "v_mov_b64 v[86:87], 0\n"
      "v_mov_b64 v[88:89], 0\n"
      "v_mov_b64 v[90:91], 0\n"
      "v_mov_b64 v[92:93], 0\n"
      "v_mov_b64 v[94:95], 0\n"
      "v_cndmask_b32 v144, 0, v144, vcc\n"
      "v_and_b32 v96, 8, v0\n"
      "v_and_b32 v97, 7, v0\n"
      "v_lshrrev_b32 v98, 3, v0\n"
      "v_lshl_or_b32 v97, v97, 3, v98\n"
      "s_lshl_b32 s0, s3, 2\n"
      "s_add_u32 s0, s0, 159744\n"
      "v_lshl_add_u32 v146, v97, 5, s0\n"
      "v_lshlrev_b32 v96, 22, v96\n"
      "v_add_u32 v145, 0x9b800000, v96\n"
      "s_waitcnt lgkmcnt(0)\n"
      "s_add_u32 s36, s36, 1\n"
      "s_addc_u32 s37, s37, 0\n"
      "v_mov_b32 v51, s36\n"
      "s_waitcnt vmcnt(7)\n"
      "v_lshl_or_b32 v16, v18, 16, v16\n"
      "v_lshl_or_b32 v17, v19, 16, v17\n"
      "v_lshl_or_b32 v16, v17, 4, v16\n"
      "s_waitcnt vmcnt(6)\n"
      "v_lshl_or_b32 v20, v22, 16, v20\n"
      "v_lshl_or_b32 v21, v23, 16, v21\n"
      "v_lshl_or_b32 v20, v21, 4, v20\n"
      "v_lshl_or_b32 v16, v20, 8, v16\n"
      "s_waitcnt vmcnt(5)\n"
      "v_lshl_or_b32 v24, v26, 16, v24\n"
      "v_lshl_or_b32 v25, v27, 16, v25\n"
      "v_lshl_or_b32 v24, v25, 4, v24\n"
      "s_waitcnt vmcnt(4)\n"
      "v_lshl_or_b32 v28, v30, 16, v28\n"
      "v_lshl_or_b32 v29, v31, 16, v29\n"
      "v_lshl_or_b32 v28, v29, 4, v28\n"
      "v_lshl_or_b32 v24, v28, 8, v24\n"
      "s_waitcnt vmcnt(3)\n"
      "v_lshl_or_b32 v32, v34, 16, v32\n"
      "v_lshl_or_b32 v33, v35, 16, v33\n"
      "v_lshl_or_b32 v32, v33, 4, v32\n"
      "s_waitcnt vmcnt(2)\n"
      "v_lshl_or_b32 v36, v38, 16, v36\n"
      "v_lshl_or_b32 v37, v39, 16, v37\n"
      "v_lshl_or_b32 v36, v37, 4, v36\n"
      "v_lshl_or_b32 v32, v36, 8, v32\n"
      "v_mov_b32_dpp v17, v16 quad_perm:[1,0,3,2] row_mask:0xf bank_mask:0xf bound_ctrl:1\n"
      "v_mov_b32_dpp v25, v24 quad_perm:[1,0,3,2] row_mask:0xf bank_mask:0xf bound_ctrl:1\n"
      "v_mov_b32_dpp v33, v32 quad_perm:[1,0,3,2] row_mask:0xf bank_mask:0xf bound_ctrl:1\n"
      "v_perm_b32 v16, v17, v16, v40\n"
      "v_perm_b32 v24, v25, v24, v40\n"
      "v_perm_b32 v32, v33, v32, v40\n"
      "v_mov_b32_dpp v17, v16 quad_perm:[2,3,0,1] row_mask:0xf bank_mask:0xf bound_ctrl:1\n"
      "v_mov_b32_dpp v25, v24 quad_perm:[2,3,0,1] row_mask:0xf bank_mask:0xf bound_ctrl:1\n"
      "v_mov_b32_dpp v33, v32 quad_perm:[2,3,0,1] row_mask:0xf bank_mask:0xf bound_ctrl:1\n"
      "v_alignbit_b32 v17, v17, v17, v44\n"
      "v_alignbit_b32 v25, v25, v25, v44\n"
      "v_alignbit_b32 v33, v33, v33, v44\n"
      "v_bfi_b32 v16, v41, v16, v17\n"
      "ds_bpermute_b32 v48, v42, v16\n"
      "v_bfi_b32 v24, v41, v24, v25\n"
      "ds_bpermute_b32 v49, v42, v24\n"
      "v_bfi_b32 v32, v41, v32, v33\n"
      "ds_bpermute_b32 v50, v42, v32\n"
      "s_waitcnt lgkmcnt(0)\n"
      "buffer_store_dwordx4 v[48:51], v43, s[24:27], s54 offen sc1\n"
      "s_barrier\n"
      "s_cmp_eq_u32 s40, 8\n"
      "s_cbranch_scc1 .Lq_pkend\n"
      "buffer_load_dwordx4 v[32:35], v11, s[32:35], 0 offen\n"
      "buffer_load_dwordx4 v[36:39], v11, s[32:35], s47 offen\n"
      "buffer_load_dwordx4 v[40:43], v11, s[32:35], s48 offen\n"
      "buffer_load_dwordx4 v[44:47], v11, s[32:35], s49 offen\n"
      "buffer_load_dwordx4 v[48:51], v12, s[32:35], 0 offen\n"
      "buffer_load_dwordx4 v[52:55], v12, s[32:35], s47 offen\n"
      "buffer_load_dwordx4 v[56:59], v12, s[32:35], s48 offen\n"
      "buffer_load_dwordx4 v[60:63], v12, s[32:35], s49 offen\n"
      "s_setprio 1\n"
      "s_waitcnt vmcnt(9)\n"
      "s_branch .Lq_stage\n"
      ".Lq_vw:\n"
      "s_movk_i32 s43, 2752\n"
      "s_and_b32 s7, s2, 1\n"
      "s_and_b32 s0, s2, 6\n"
      "s_lshl_b32 s0, s0, 2\n"
      "s_lshr_b32 s0, 0x100b0600, s0\n"
      "s_and_b32 s0, s0, 31\n"
      "s_lshr_b32 s41, s5, 2\n"
      "s_add_u32 s41, s41, s0\n"
      "s_sub_u32 s41, s41, 2\n"
      "s_mul_i32 s42, s6, 5\n"
      "s_add_u32 s42, s42, s3\n"
      "v_and_b32 v8, 56, v0\n"
      "v_and_b32 v9, 0x70, v1\n"
      "s_lshl_b32 s0, s41, 7\n"
      "s_mul_i32 s1, s7, 2688\n"
      "s_add_u32 s0, s0, s1\n"
      "v_mad_u32_u24 v8, v8, s43, v9\n"
      "v_and_b32 v10, 24, v0\n"
      "v_add_u32 v8, s0, v8\n"
      "s_add_u32 s0, s42, 20\n"
      "v_add_u32 v10, s0, v10\n"
      "v_subrev_u32 v11, 22, v10\n"
      "v_subrev_u32 v12, 44, v10\n"
      "v_min_u32 v10, v10, v11\n"
      "v_min_u32 v10, v10, v12\n"
      "v_lshl_or_b32 v9, v10, 7, v9\n"
      "v_min_u32 v9, 0xab0, v9\n"
      "s_mul_i32 s0, s6, 2752\n"
      "v_add_u32 v9, s0, v9\n"
      "s_movk_i32 s47, 5504\n"
      "s_movk_i32 s48, 11008\n"
      "s_movk_i32 s49, 16512\n"
      "s_mov_b32 s34, 3786752\n"
      "s_mov_b32 s35, 0x20000\n"
      "s_add_u32 s44, s42, -2\n"
      "s_sub_u32 s0, s44, 22\n"
      "s_min_u32 s44, s44, s0\n"
      "s_mul_i32 s0, s44, 176128\n"
      "v_add_u32 v10, s0, v8\n"
      "s_add_u32 s45, s42, 6\n"
      "s_sub_u32 s0, s45, 22\n"
      "s_min_u32 s45, s45, s0\n"
      "s_mul_i32 s0, s45, 176128\n"
      "v_add_u32 v11, s0, v8\n"
      "s_waitcnt lgkmcnt(0)\n"
      "s_mov_b64 s[32:33], s[18:19]\n"
      "s_mov_b32 exec_hi, 0\n"
      "global_load_dwordx4 v[4:7], v9, s[12:13]\n"
      "s_mov_b32 exec_hi, -1\n"
      "s_mov_b32 exec_lo, 0\n"
      "global_load_dwordx4 v[4:7], v9, s[14:15]\n"
      "s_mov_b32 exec_lo, -1\n"
      "buffer_load_dwordx4 v[16:19], v10, s[32:35], 0 offen\n"
      "buffer_load_dwordx4 v[20:23], v10, s[32:35], s47 offen\n"
      "buffer_load_dwordx4 v[24:27], v10, s[32:35], s48 offen\n"
      "buffer_load_dwordx4 v[28:31], v10, s[32:35], s49 offen\n"
      "s_cmp_lt_u32 s3, 4\n"
      "s_cbranch_scc1 .Lq_vw1\n"
      "buffer_load_dwordx4 v[32:35], v11, s[32:35], 0 offen\n"
      "buffer_load_dwordx4 v[36:39], v11, s[32:35], s47 offen\n"
      "buffer_load_dwordx4 v[40:43], v11, s[32:35], s48 offen\n"
      "buffer_load_dwordx4 v[44:47], v11, s[32:35], s49 offen\n"
      ".Lq_vw1:\n"
      "s_load_dwordx2 s[36:37], s[22:23], 0x0\n"
      "s_mov_b64 s[24:25], s[22:23]\n"
      "s_mov_b32 s26, 0xc00000\n"
      "s_mov_b32 s27, 0x20000\n"
      "s_add_u32 s46, s42, 14\n"
      "s_sub_u32 s0, s46, 22\n"
      "s_min_u32 s46, s46, s0\n"
      "s_mul_i32 s0, s46, 176128\n"
      "v_add_u32 v12, s0, v8\n"
      "v_bfe_u32 v96, v0, 2, 1\n"
      "v_and_b32 v97, 1, v0\n"
      "v_lshlrev_b32 v98, 5, v0\n"
      "v_mul_u32_u24 v96, 45072, v96\n"
      "v_mul_u32_u24 v97, 288, v97\n"
      "v_and_b32 v98, 0x640, v98\n"
      "v_and_b32 v99, 8, v0\n"
      "v_or3_b32 v96, v96, v97, v98\n"
      "v_or_b32 v138, v96, v99\n"
      "v_xor_b32 v139, 16, v138\n"
      "v_xor_b32 v140, 32, v138\n"
      "v_xor_b32 v141, 48, v138\n"
      "s_mul_i32 s50, s7, 0x02020202\n"
      "s_add_u32 s50, s50, 0x05040100\n"
      "s_mul_i32 s51, s7, 0x0202\n"
      "s_lshl_b32 s52, s51, 16\n"
      "s_add_u32 s51, s51, 0x0c0c0100\n"
      "s_add_u32 s52, s52, 0x01000c0c\n"
      "s_lshl_b32 s0, s3, 10\n"
      "s_add_u32 s0, s0, 151552\n"
      "v_add_u32 v142, s0, v1\n"
      "v_and_b32 v143, 56, v0\n"
      "v_lshl_add_u32 v143, v143, 1, s0\n"
      "v_and_b32 v97, 1, v0\n"
      "v_mul_u32_u24 v97, 0x200, v97\n"
      "v_sub_u32 v144, 0x2400, v97\n"
      "v_cmp_gt_u32 vcc, 32, v0\n"
      "v_lshl_or_b32 v144, v144, 16, v144\n"
      "v_mov_b64 v[64:65], 0\n"
      "v_mov_b64 v[66:67], 0\n"
      "v_mov_b64 v[68:69], 0\n"
      "v_mov_b64 v[70:71], 0\n"
      "v_mov_b64 v[72:73], 0\n"
      "v_mov_b64 v[74:75], 0\n"
      "v_mov_b64 v[76:77], 0\n"
      "v_mov_b64 v[78:79], 0\n"
      "v_mov_b64 v[80:81], 0\n"
      "v_mov_b64 v[82:83], 0\n"
      "v_mov_b64 v[84:85], 0\n"
      "v_mov_b64 v[86:87], 0\n"
      "v_mov_b64 v[88:89], 0\n"
      "v_mov_b64 v[90:91], 0\n"
      "v_mov_b64 v[92:93], 0\n"
      "v_mov_b64 v[94:95], 0\n"
      "v_cndmask_b32 v144, 0, v144, vcc\n"
      "v_and_b32 v96, 8, v0\n"
      "v_and_b32 v97, 7, v0\n"
      "v_lshrrev_b32 v98, 3, v0\n"
      "v_lshl_or_b32 v97, v97, 3, v98\n"
      "s_lshl_b32 s0, s3, 2\n"
      "s_add_u32 s0, s0, 159744\n"
      "v_lshl_add_u32 v146, v97, 5, s0\n"
      "v_lshlrev_b32 v96, 22, v96\n"
      "v_add_u32 v145, 0x9b800000, v96\n"
      "s_cmp_ge_u32 s3, 4\n"
      "s_barrier\n"
      "s_cbranch_scc1 .Lq_vw2\n"
      "buffer_load_dwordx4 v[32:35], v11, s[32:35], 0 offen\n"
      "buffer_load_dwordx4 v[36:39], v11, s[32:35], s47 offen\n"
      "buffer_load_dwordx4 v[40:43], v11, s[32:35], s48 offen\n"
      "buffer_load_dwordx4 v[44:47], v11, s[32:35], s49 offen\n"
      "buffer_load_dwordx4 v[48:51], v12, s[32:35], 0 offen\n"
      "buffer_load_dwordx4 v[52:55], v12, s[32:35], s47 offen\n"
      "buffer_load_dwordx4 v[56:59], v12, s[32:35], s48 offen\n"
      "buffer_load_dwordx4 v[60:63], v12, s[32:35], s49 offen\n"
      "s_waitcnt lgkmcnt(0)\n"
      "s_add_u32 s36, s36, 1\n"
      "s_addc_u32 s37, s37, 0\n"
      "s_waitcnt vmcnt(12)\n"
      "s_branch .Lq_stage\n"
      ".Lq_vw2:\n"
      "s_waitcnt lgkmcnt(0)\n"
      "s_add_u32 s36, s36, 1\n"
      "s_addc_u32 s37, s37, 0\n"
      "s_waitcnt vmcnt(8)\n"
      ".Lq_stage:\n"
      "v_add_u32 v4, v144, v4\n"
      "v_add_u32 v5, v144, v5\n"
      "v_add_u32 v6, v144, v6\n"
      "v_add_u32 v7, v144, v7\n"
      "ds_write_b128 v142, v[4:7]\n"
      "ds_read_b128 v[148:151], v143 offset:0\n"
      "ds_read_b128 v[152:155], v143 offset:512\n"
      "ds_read_b128 v[156:159], v143 offset:128\n"
      "ds_read_b128 v[160:163], v143 offset:640\n"
      "ds_read_b128 v[164:167], v143 offset:256\n"
      "ds_read_b128 v[168:171], v143 offset:768\n"
      "s_cmp_lt_u32 s3, 2\n"
      "s_waitcnt lgkmcnt(0)\n"
      "s_cbranch_scc1 .Lq_c1\n"
      "s_lshl_b32 s0, s44, 11\n"
      "v_add_u32 v172, s0, v138\n"
      "v_add_u32 v173, s0, v139\n"
      "v_add_u32 v174, s0, v140\n"
      "v_add_u32 v175, s0, v141\n"
      "v_perm_b32 v108, v148, v148, s51\n"
      "v_perm_b32 v112, v148, v148, s52\n"
      "v_perm_b32 v109, v149, v149, s51\n"
      "v_perm_b32 v113, v149, v149, s52\n"
      "v_perm_b32 v110, v150, v150, s51\n"
      "v_perm_b32 v114, v150, v150, s52\n"
      "v_perm_b32 v111, v151, v151, s51\n"
      "v_perm_b32 v115, v151, v151, s52\n"
      "v_perm_b32 v116, v153, v152, s50\n"
      "v_perm_b32 v117, v155, v154, s50\n"
      "s_waitcnt vmcnt(8)\n"
      "s_cmp_lt_u32 s3, 4\n"
      "s_cbranch_scc1 .Lq_c0a\n"
      "s_waitcnt vmcnt(4)\n"
      ".Lq_c0a:\n"
      "v_dot2_f32_bf16 v96, v16, v108, 0\n"
      "v_dot2_f32_bf16 v97, v20, v109, 0\n"
      "v_dot2_f32_bf16 v98, v24, v110, 0\n"
      "v_dot2_f32_bf16 v99, v28, v111, 0\n"
      "v_dot2_f32_bf16 v100, v16, v112, 0\n"
      "v_dot2_f32_bf16 v101, v20, v113, 0\n"
      "v_dot2_f32_bf16 v102, v24, v114, 0\n"
      "v_dot2_f32_bf16 v103, v28, v115, 0\n"
      "v_cvt_pk_bf16_f32 v104, v96, v97\n"
      "v_cvt_pk_bf16_f32 v105, v98, v99\n"
      "v_cvt_pk_bf16_f32 v106, v100, v101\n"
      "v_cvt_pk_bf16_f32 v107, v102, v103\n"
      "ds_write_b64 v172, v[104:105] offset:0\n"
      "ds_write_b64 v173, v[106:107] offset:0\n"
      "v_mfma_f32_4x4x4_16b_bf16 v[64:67], v[116:117], v[104:105], v[64:67]\n"
      "v_mfma_f32_4x4x4_16b_bf16 v[68:71], v[116:117], v[106:107], v[68:71]\n"
      "v_dot2_f32_bf16 v96, v17, v108, 0\n"
      "v_dot2_f32_bf16 v97, v21, v109, 0\n"
      "v_dot2_f32_bf16 v98, v25, v110, 0\n"
      "v_dot2_f32_bf16 v99, v29, v111, 0\n"
      "v_dot2_f32_bf16 v100, v17, v112, 0\n"
      "v_dot2_f32_bf16 v101, v21, v113, 0\n"
      "v_dot2_f32_bf16 v102, v25, v114, 0\n"
      "v_dot2_f32_bf16 v103, v29, v115, 0\n"
      "v_cvt_pk_bf16_f32 v104, v96, v97\n"
      "v_cvt_pk_bf16_f32 v105, v98, v99\n"
      "v_cvt_pk_bf16_f32 v106, v100, v101\n"
      "v_cvt_pk_bf16_f32 v107, v102, v103\n"
      "ds_write_b64 v174, v[104:105] offset:0\n"
      "ds_write_b64 v175, v[106:107] offset:0\n"
      "v_mfma_f32_4x4x4_16b_bf16 v[72:75], v[116:117], v[104:105], v[72:75]\n"
      "v_mfma_f32_4x4x4_16b_bf16 v[76:79], v[116:117], v[106:107], v[76:79]\n"
      "v_dot2_f32_bf16 v96, v18, v108, 0\n"
      "v_dot2_f32_bf16 v97, v22, v109, 0\n"
      "v_dot2_f32_bf16 v98, v26, v110, 0\n"
      "v_dot2_f32_bf16 v99, v30, v111, 0\n"
      "v_dot2_f32_bf16 v100, v18, v112, 0\n"
      "v_dot2_f32_bf16 v101, v22, v113, 0\n"
      "v_dot2_f32_bf16 v102, v26, v114, 0\n"
      "v_dot2_f32_bf16 v103, v30, v115, 0\n"
      "v_cvt_pk_bf16_f32 v104, v96, v97\n"
      "v_cvt_pk_bf16_f32 v105, v98, v99\n"
      "v_cvt_pk_bf16_f32 v106, v100, v101\n"
      "v_cvt_pk_bf16_f32 v107, v102, v103\n"
      "ds_write_b64 v172, v[104:105] offset:128\n"
      "ds_write_b64 v173, v[106:107] offset:128\n"
      "v_mfma_f32_4x4x4_16b_bf16 v[80:83], v[116:117], v[104:105], v[80:83]\n"
      "v_mfma_f32_4x4x4_16b_bf16 v[84:87], v[116:117], v[106:107], v[84:87]\n"
      "v_dot2_f32_bf16 v96, v19, v108, 0\n"
      "v_dot2_f32_bf16 v97, v23, v109, 0\n"
      "v_dot2_f32_bf16 v98, v27, v110, 0\n"
      "v_dot2_f32_bf16 v99, v31, v111, 0\n"
      "v_dot2_f32_bf16 v100, v19, v112, 0\n"
      "v_dot2_f32_bf16 v101, v23, v113, 0\n"
      "v_dot2_f32_bf16 v102, v27, v114, 0\n"
      "v_dot2_f32_bf16 v103, v31, v115, 0\n"
      "v_cvt_pk_bf16_f32 v104, v96, v97\n"
      "v_cvt_pk_bf16_f32 v105, v98, v99\n"
      "v_cvt_pk_bf16_f32 v106, v100, v101\n"
      "v_cvt_pk_bf16_f32 v107, v102, v103\n"
      "ds_write_b64 v174, v[104:105] offset:128\n"
      "ds_write_b64 v175, v[106:107] offset:128\n"
      "v_mfma_f32_4x4x4_16b_bf16 v[88:91], v[116:117], v[104:105], v[88:91]\n"
      "v_mfma_f32_4x4x4_16b_bf16 v[92:95], v[116:117], v[106:107], v[92:95]\n"
      "s_cmp_lt_u32 s3, 4\n"
      "s_cbranch_scc1 .Lq_c1\n"
      "buffer_load_dwordx4 v[48:51], v12, s[32:35], 0 offen\n"
      "buffer_load_dwordx4 v[52:55], v12, s[32:35], s47 offen\n"
      "buffer_load_dwordx4 v[56:59], v12, s[32:35], s48 offen\n"
      "buffer_load_dwordx4 v[60:63], v12, s[32:35], s49 offen\n"
      ".Lq_c1:\n"
      "s_lshl_b32 s0, s45, 11\n"
      "v_add_u32 v172, s0, v138\n"
      "v_add_u32 v173, s0, v139\n"
      "v_add_u32 v174, s0, v140\n"
      "v_add_u32 v175, s0, v141\n"
      "v_perm_b32 v118, v156, v156, s51\n"
      "v_perm_b32 v122, v156, v156, s52\n"
      "v_perm_b32 v119, v157, v157, s51\n"
      "v_perm_b32 v123, v157, v157, s52\n"
      "v_perm_b32 v120, v158, v158, s51\n"
      "v_perm_b32 v124, v158, v158, s52\n"
      "v_perm_b32 v121, v159, v159, s51\n"
      "v_perm_b32 v125, v159, v159, s52\n"
      "v_perm_b32 v126, v161, v160, s50\n"
      "v_perm_b32 v127, v163, v162, s50\n"
      "s_cmp_ge_u32 s3, 4\n"
      "s_cbranch_scc1 .Lq_c1m\n"
      "v_and_b32 v2, 4, v0\n"
      "v_and_b32 v3, 8, v0\n"
      "v_lshl_or_b32 v4, v2, 2, v3\n"
      "v_bfe_u32 v5, v0, 4, 1\n"
      "v_lshl_or_b32 v4, v5, 2, v4\n"
      "v_lshrrev_b32 v5, 1, v0\n"
      "v_xor_b32 v5, v5, v0\n"
      "v_and_b32 v5, 2, v5\n"
      "v_and_b32 v6, 33, v0\n"
      "v_or3_b32 v4, v4, v5, v6\n"
      "v_and_b32 v5, 19, v0\n"
      "v_lshl_or_b32 v5, v2, 1, v5\n"
      "v_lshrrev_b32 v3, 1, v3\n"
      "v_or_b32 v5, v5, v3\n"
      "v_lshlrev_b32 v2, 4, v4\n"
      "v_xor_b32 v3, 16, v2\n"
      "v_add_u32 v3, 45056, v3\n"
      "v_lshlrev_b32 v14, 5, v5\n"
      "v_add_u32 v14, 159744, v14\n"
      "s_lshl_b32 s41, s41, 1\n"
      "s_sub_u32 s41, s41, s7\n"
      "s_lshl_b32 s0, s41, 2\n"
      "s_add_u32 s0, s0, s6\n"
      "s_lshl_b32 s0, s0, 2\n"
      "s_add_u32 s0, s0, s3\n"
      "s_mul_i32 s49, s0, 3072\n"
      "s_add_u32 s49, s49, 6291456\n"
      "s_lshl_b32 s0, s0, 11\n"
      "s_add_u32 s40, s0, 3211264\n"
      "v_mov_b32 v7, s36\n"
      "v_mov_b32 v11, s36\n"
      "v_mov_b32 v15, s36\n"
      "s_movk_i32 s47, 0x3f80\n"
      "s_mov_b32 s48, 0x3f800000\n"
      "s_lshl_b32 s0, s6, 2\n"
      "s_add_u32 s0, s0, s3\n"
      "s_lshl_b32 s0, s0, 1\n"
      "s_add_u32 s0, s0, s7\n"
      "s_mul_i32 s53, s0, 15360\n"
      "s_add_u32 s53, s53, 65536\n"
      "s_add_u32 s54, s53, 4096\n"
      "s_add_u32 s55, s53, 8192\n"
      "s_add_u32 s56, s53, 12288\n"
      ".Lq_c1m:\n"
      "s_waitcnt vmcnt(4)\n"
      "v_dot2_f32_bf16 v96, v32, v118, 0\n"
      "v_dot2_f32_bf16 v97, v36, v119, 0\n"
      "v_dot2_f32_bf16 v98, v40, v120, 0\n"
      "v_dot2_f32_bf16 v99, v44, v121, 0\n"
      "v_dot2_f32_bf16 v100, v32, v122, 0\n"
      "v_dot2_f32_bf16 v101, v36, v123, 0\n"
      "v_dot2_f32_bf16 v102, v40, v124, 0\n"
      "v_dot2_f32_bf16 v103, v44, v125, 0\n"
      "v_cvt_pk_bf16_f32 v104, v96, v97\n"
      "v_cvt_pk_bf16_f32 v105, v98, v99\n"
      "v_cvt_pk_bf16_f32 v106, v100, v101\n"
      "v_cvt_pk_bf16_f32 v107, v102, v103\n"
      "ds_write_b64 v172, v[104:105] offset:0\n"
      "ds_write_b64 v173, v[106:107] offset:0\n"
      "v_mfma_f32_4x4x4_16b_bf16 v[64:67], v[126:127], v[104:105], v[64:67]\n"
      "v_mfma_f32_4x4x4_16b_bf16 v[68:71], v[126:127], v[106:107], v[68:71]\n"
      "v_dot2_f32_bf16 v96, v33, v118, 0\n"
      "v_dot2_f32_bf16 v97, v37, v119, 0\n"
      "v_dot2_f32_bf16 v98, v41, v120, 0\n"
      "v_dot2_f32_bf16 v99, v45, v121, 0\n"
      "v_dot2_f32_bf16 v100, v33, v122, 0\n"
      "v_dot2_f32_bf16 v101, v37, v123, 0\n"
      "v_dot2_f32_bf16 v102, v41, v124, 0\n"
      "v_dot2_f32_bf16 v103, v45, v125, 0\n"
      "v_cvt_pk_bf16_f32 v104, v96, v97\n"
      "v_cvt_pk_bf16_f32 v105, v98, v99\n"
      "v_cvt_pk_bf16_f32 v106, v100, v101\n"
      "v_cvt_pk_bf16_f32 v107, v102, v103\n"
      "ds_write_b64 v174, v[104:105] offset:0\n"
      "ds_write_b64 v175, v[106:107] offset:0\n"
      "v_mfma_f32_4x4x4_16b_bf16 v[72:75], v[126:127], v[104:105], v[72:75]\n"
      "v_mfma_f32_4x4x4_16b_bf16 v[76:79], v[126:127], v[106:107], v[76:79]\n"
      "v_dot2_f32_bf16 v96, v34, v118, 0\n"
      "v_dot2_f32_bf16 v97, v38, v119, 0\n"
      "v_dot2_f32_bf16 v98, v42, v120, 0\n"
      "v_dot2_f32_bf16 v99, v46, v121, 0\n"
      "v_dot2_f32_bf16 v100, v34, v122, 0\n"
      "v_dot2_f32_bf16 v101, v38, v123, 0\n"
      "v_dot2_f32_bf16 v102, v42, v124, 0\n"
      "v_dot2_f32_bf16 v103, v46, v125, 0\n"
      "v_cvt_pk_bf16_f32 v104, v96, v97\n"
      "v_cvt_pk_bf16_f32 v105, v98, v99\n"
      "v_cvt_pk_bf16_f32 v106, v100, v101\n"
      "v_cvt_pk_bf16_f32 v107, v102, v103\n"
      "ds_write_b64 v172, v[104:105] offset:128\n"
      "ds_write_b64 v173, v[106:107] offset:128\n"
      "v_mfma_f32_4x4x4_16b_bf16 v[80:83], v[126:127], v[104:105], v[80:83]\n"
      "v_mfma_f32_4x4x4_16b_bf16 v[84:87], v[126:127], v[106:107], v[84:87]\n"
      "v_dot2_f32_bf16 v96, v35, v118, 0\n"
      "v_dot2_f32_bf16 v97, v39, v119, 0\n"
      "v_dot2_f32_bf16 v98, v43, v120, 0\n"
      "v_dot2_f32_bf16 v99, v47, v121, 0\n"
      "v_dot2_f32_bf16 v100, v35, v122, 0\n"
      "v_dot2_f32_bf16 v101, v39, v123, 0\n"
      "v_dot2_f32_bf16 v102, v43, v124, 0\n"
      "v_dot2_f32_bf16 v103, v47, v125, 0\n"
      "v_cvt_pk_bf16_f32 v104, v96, v97\n"
      "v_cvt_pk_bf16_f32 v105, v98, v99\n"
      "v_cvt_pk_bf16_f32 v106, v100, v101\n"
      "v_cvt_pk_bf16_f32 v107, v102, v103\n"
      "ds_write_b64 v174, v[104:105] offset:128\n"
      "ds_write_b64 v175, v[106:107] offset:128\n"
      "v_mfma_f32_4x4x4_16b_bf16 v[88:91], v[126:127], v[104:105], v[88:91]\n"
      "v_mfma_f32_4x4x4_16b_bf16 v[92:95], v[126:127], v[106:107], v[92:95]\n"
      "s_lshl_b32 s0, s46, 11\n"
      "v_add_u32 v172, s0, v138\n"
      "v_add_u32 v173, s0, v139\n"
      "v_add_u32 v174, s0, v140\n"
      "v_add_u32 v175, s0, v141\n"
      "v_perm_b32 v128, v164, v164, s51\n"
      "v_perm_b32 v132, v164, v164, s52\n"
      "v_perm_b32 v129, v165, v165, s51\n"
      "v_perm_b32 v133, v165, v165, s52\n"
      "v_perm_b32 v130, v166, v166, s51\n"
      "v_perm_b32 v134, v166, v166, s52\n"
      "v_perm_b32 v131, v167, v167, s51\n"
      "v_perm_b32 v135, v167, v167, s52\n"
      "v_perm_b32 v136, v169, v168, s50\n"
      "v_perm_b32 v137, v171, v170, s50\n"
      "s_cmp_ge_u32 s3, 4\n"
      "s_cbranch_scc1 .Lq_c2a\n"
      "buffer_load_dwordx4 v[196:199], v1, s[24:27], s53 offen offset:0 sc1\n"
      "buffer_load_dwordx4 v[200:203], v1, s[24:27], s53 offen offset:1024 sc1\n"
      "s_waitcnt vmcnt(2)\n"
      "s_branch .Lq_c2b\n"
      ".Lq_c2a:\n"
      "s_waitcnt vmcnt(0)\n"
      ".Lq_c2b:\n"
      "v_dot2_f32_bf16 v96, v48, v128, 0\n"
      "v_dot2_f32_bf16 v97, v52, v129, 0\n"
      "v_dot2_f32_bf16 v98, v56, v130, 0\n"
      "v_dot2_f32_bf16 v99, v60, v131, 0\n"
      "v_dot2_f32_bf16 v100, v48, v132, 0\n"
      "v_dot2_f32_bf16 v101, v52, v133, 0\n"
      "v_dot2_f32_bf16 v102, v56, v134, 0\n"
      "v_dot2_f32_bf16 v103, v60, v135, 0\n"
      "v_cvt_pk_bf16_f32 v104, v96, v97\n"
      "v_cvt_pk_bf16_f32 v105, v98, v99\n"
      "v_cvt_pk_bf16_f32 v106, v100, v101\n"
      "v_cvt_pk_bf16_f32 v107, v102, v103\n"
      "ds_write_b64 v172, v[104:105] offset:0\n"
      "ds_write_b64 v173, v[106:107] offset:0\n"
      "v_mfma_f32_4x4x4_16b_bf16 v[64:67], v[136:137], v[104:105], v[64:67]\n"
      "v_mfma_f32_4x4x4_16b_bf16 v[68:71], v[136:137], v[106:107], v[68:71]\n"
      "s_cbranch_scc1 .Lq_q20\n"
      "buffer_load_dwordx4 v[204:207], v1, s[24:27], s53 offen offset:2048 sc1\n"
      "buffer_load_dwordx4 v[208:211], v1, s[24:27], s53 offen offset:3072 sc1\n"
      ".Lq_q20:\n"
      "v_dot2_f32_bf16 v96, v49, v128, 0\n"
      "v_dot2_f32_bf16 v97, v53, v129, 0\n"
      "v_dot2_f32_bf16 v98, v57, v130, 0\n"
      "v_dot2_f32_bf16 v99, v61, v131, 0\n"
      "v_dot2_f32_bf16 v100, v49, v132, 0\n"
      "v_dot2_f32_bf16 v101, v53, v133, 0\n"
      "v_dot2_f32_bf16 v102, v57, v134, 0\n"
      "v_dot2_f32_bf16 v103, v61, v135, 0\n"
      "v_cvt_pk_bf16_f32 v104, v96, v97\n"
      "v_cvt_pk_bf16_f32 v105, v98, v99\n"
      "v_cvt_pk_bf16_f32 v106, v100, v101\n"
      "v_cvt_pk_bf16_f32 v107, v102, v103\n"
      "ds_write_b64 v174, v[104:105] offset:0\n"
      "ds_write_b64 v175, v[106:107] offset:0\n"
      "v_mfma_f32_4x4x4_16b_bf16 v[72:75], v[136:137], v[104:105], v[72:75]\n"
      "v_mfma_f32_4x4x4_16b_bf16 v[76:79], v[136:137], v[106:107], v[76:79]\n"
      "s_cbranch_scc1 .Lq_q21\n"
      "buffer_load_dwordx4 v[212:215], v1, s[24:27], s54 offen offset:0 sc1\n"
      "buffer_load_dwordx4 v[216:219], v1, s[24:27], s54 offen offset:1024 sc1\n"
      "buffer_load_dwordx4 v[220:223], v1, s[24:27], s54 offen offset:2048 sc1\n"
      ".Lq_q21:\n"
      "v_dot2_f32_bf16 v96, v50, v128, 0\n"
      "v_dot2_f32_bf16 v97, v54, v129, 0\n"
      "v_dot2_f32_bf16 v98, v58, v130, 0\n"
      "v_dot2_f32_bf16 v99, v62, v131, 0\n"
      "v_dot2_f32_bf16 v100, v50, v132, 0\n"
      "v_dot2_f32_bf16 v101, v54, v133, 0\n"
      "v_dot2_f32_bf16 v102, v58, v134, 0\n"
      "v_dot2_f32_bf16 v103, v62, v135, 0\n"
      "v_cvt_pk_bf16_f32 v104, v96, v97\n"
      "v_cvt_pk_bf16_f32 v105, v98, v99\n"
      "v_cvt_pk_bf16_f32 v106, v100, v101\n"
      "v_cvt_pk_bf16_f32 v107, v102, v103\n"
      "ds_write_b64 v172, v[104:105] offset:128\n"
      "ds_write_b64 v173, v[106:107] offset:128\n"
      "v_mfma_f32_4x4x4_16b_bf16 v[80:83], v[136:137], v[104:105], v[80:83]\n"
      "v_mfma_f32_4x4x4_16b_bf16 v[84:87], v[136:137], v[106:107], v[84:87]\n"
      "s_cbranch_scc1 .Lq_q22\n"
      "buffer_load_dwordx4 v[224:227], v1, s[24:27], s54 offen offset:3072 sc1\n"
      "buffer_load_dwordx4 v[228:231], v1, s[24:27], s55 offen offset:0 sc1\n"
      "buffer_load_dwordx4 v[232:235], v1, s[24:27], s55 offen offset:1024 sc1\n"
      ".Lq_q22:\n"
      "v_dot2_f32_bf16 v96, v51, v128, 0\n"
      "v_dot2_f32_bf16 v97, v55, v129, 0\n"
      "v_dot2_f32_bf16 v98, v59, v130, 0\n"
      "v_dot2_f32_bf16 v99, v63, v131, 0\n"
      "v_dot2_f32_bf16 v100, v51, v132, 0\n"
      "v_dot2_f32_bf16 v101, v55, v133, 0\n"
      "v_dot2_f32_bf16 v102, v59, v134, 0\n"
      "v_dot2_f32_bf16 v103, v63, v135, 0\n"
      "v_cvt_pk_bf16_f32 v104, v96, v97\n"
      "v_cvt_pk_bf16_f32 v105, v98, v99\n"
      "v_cvt_pk_bf16_f32 v106, v100, v101\n"
      "v_cvt_pk_bf16_f32 v107, v102, v103\n"
      "ds_write_b64 v174, v[104:105] offset:128\n"
      "ds_write_b64 v175, v[106:107] offset:128\n"
      "v_mfma_f32_4x4x4_16b_bf16 v[88:91], v[136:137], v[104:105], v[88:91]\n"
      "v_mfma_f32_4x4x4_16b_bf16 v[92:95], v[136:137], v[106:107], v[92:95]\n"
      "v_cmp_eq_u32 vcc, 0x9b800000, v145\n"
      "s_nop 5\n"
      "v_permlane32_swap_b32 v64, v80\n"
      "v_permlane32_swap_b32 v68, v84\n"
      "v_permlane32_swap_b32 v72, v88\n"
      "v_permlane32_swap_b32 v76, v92\n"
      "v_add_f32 v64, v64, v80\n"
      "v_add_f32 v68, v68, v84\n"
      "v_add_f32 v72, v72, v88\n"
      "v_add_f32 v76, v76, v92\n"
      "s_nop 0\n"
      "v_permlane16_swap_b32 v64, v72\n"
      "v_permlane16_swap_b32 v68, v76\n"
      "s_nop 0\n"
      "v_add_f32 v64, v64, v72\n"
      "v_add_f32 v68, v68, v76\n"
      "v_mul_f32 v64, v64, v145\n"
      "v_mul_f32 v68, v68, v145\n"
      "s_cmp_ge_u32 s3, 4\n"
      "s_nop 0\n"
      "v_add_f32_dpp v64, v64, v64 row_ror:8 row_mask:0xf bank_mask:0xf\n"
      "v_add_f32_dpp v68, v68, v68 row_ror:8 row_mask:0xf bank_mask:0xf\n"
      "v_cndmask_b32 v64, v68, v64, vcc\n"
      "ds_write_b32 v146, v64\n"
      "s_cbranch_scc0 .Lq_mw\n"
      "s_waitcnt lgkmcnt(0)\n"
      "s_barrier\n"
      "s_sleep 80\n"
      "s_branch .Lq_pc\n"
      ".Lq_mw:\n"
      "s_setprio 3\n"
      "s_mov_b32 s44, 0x000f000f\n"
      "s_mov_b32 s45, 0x00f000f0\n"
      "s_mov_b32 s46, 0x5e000000\n"
      "s_add_u32 s50, s49, 49152\n"
      "s_waitcnt lgkmcnt(0)\n"
      "s_barrier\n"
      "s_mov_b32 s57, 0x4000\n"
      "ds_read_b128 v[48:51], v3 offset:0\n"
      "ds_read_b128 v[52:55], v3 offset:1024\n"
      "ds_read_b128 v[56:59], v3 offset:2048\n"
      "ds_read_b128 v[60:63], v3 offset:3072\n"
      "ds_read_b128 v[64:67], v3 offset:4096\n"
      "s_waitcnt vmcnt(8)\n"
      ".Lq_q0:\n"
      "v_bitop3_b32 v4, v199, v203, s36 bitop3:0x7e\n"
      "v_cmp_ne_u32 vcc, 0, v4\n"
      "s_cbranch_vccnz .Lq_q0r\n"
      ".Lq_q0x:\n"
      "v_lshrrev_b32 v81, 8, v196\n"
      "v_and_b32 v80, s44, v196\n"
      "v_and_b32 v82, s45, v196\n"
      "v_and_b32 v83, s45, v81\n"
      "v_and_b32 v81, s44, v81\n"
      "v_lshrrev_b32 v85, 8, v197\n"
      "v_and_b32 v84, s44, v197\n"
      "v_and_b32 v86, s45, v197\n"
      "v_and_b32 v87, s45, v85\n"
      "v_and_b32 v85, s44, v85\n"
      "s_waitcnt lgkmcnt(3)\n"
      "v_mfma_f32_32x32x16_bf16 v[16:31], v[80:83], v[48:51], 0\n"
      "ds_read_b128 v[68:71], v3 offset:5120\n"
      "v_lshrrev_b32 v89, 8, v198\n"
      "v_and_b32 v88, s44, v198\n"
      "v_and_b32 v90, s45, v198\n"
      "v_and_b32 v91, s45, v89\n"
      "v_and_b32 v89, s44, v89\n"
      "buffer_load_dwordx4 v[236:239], v1, s[24:27], s55 offen offset:2048 sc1\n"
      "v_mfma_f32_32x32x16_bf16 v[16:31], v[84:87], v[52:55], v[16:31]\n"
      "ds_read_b128 v[48:51], v3 offset:6144\n"
      "v_lshrrev_b32 v93, 8, v200\n"
      "v_and_b32 v92, s44, v200\n"
      "v_and_b32 v94, s45, v200\n"
      "v_and_b32 v95, s45, v93\n"
      "v_and_b32 v93, s44, v93\n"
      "buffer_load_dwordx4 v[240:243], v1, s[24:27], s55 offen offset:3072 sc1\n"
      "s_waitcnt lgkmcnt(3)\n"
      "v_mfma_f32_32x32x16_bf16 v[16:31], v[88:91], v[56:59], v[16:31]\n"
      "ds_read_b128 v[52:55], v3 offset:7168\n"
      "v_lshrrev_b32 v97, 8, v201\n"
      "v_and_b32 v96, s44, v201\n"
      "v_and_b32 v98, s45, v201\n"
      "v_and_b32 v99, s45, v97\n"
      "v_and_b32 v97, s44, v97\n"
      "buffer_load_dwordx4 v[244:247], v1, s[24:27], s56 offen offset:0 sc1\n"
      "v_mfma_f32_32x32x16_bf16 v[16:31], v[92:95], v[60:63], v[16:31]\n"
      "ds_read_b128 v[56:59], v3 offset:8192\n"
      "v_lshrrev_b32 v101, 8, v202\n"
      "v_and_b32 v100, s44, v202\n"
      "v_and_b32 v102, s45, v202\n"
      "v_and_b32 v103, s45, v101\n"
      "v_and_b32 v101, s44, v101\n"
      "buffer_load_dwordx4 v[248:251], v1, s[24:27], s56 offen offset:1024 sc1\n"
      "s_waitcnt lgkmcnt(3)\n"
      "v_mfma_f32_32x32x16_bf16 v[16:31], v[96:99], v[64:67], v[16:31]\n"
      "ds_read_b128 v[60:63], v3 offset:9216\n"
      "s_waitcnt vmcnt(8)\n"
      ".Lq_q1:\n"
      "v_bitop3_b32 v4, v207, v211, s36 bitop3:0x7e\n"
      "v_bitop3_b32 v4, v4, v215, s36 bitop3:0xf6\n"
      "v_bitop3_b32 v4, v4, v219, s36 bitop3:0xf6\n"
      "v_cmp_ne_u32 vcc, 0, v4\n"
      "s_cbranch_vccnz .Lq_q1r\n"
      ".Lq_q1x:\n"
      "v_lshrrev_b32 v105, 8, v204\n"
      "v_and_b32 v104, s44, v204\n"
      "v_and_b32 v106, s45, v204\n"
      "v_and_b32 v107, s45, v105\n"
      "v_and_b32 v105, s44, v105\n"
      "buffer_load_dwordx4 v[252:255], v1, s[24:27], s56 offen offset:2048 sc1\n"
      "v_mfma_f32_32x32x16_bf16 v[16:31], v[100:103], v[68:71], v[16:31]\n"
      "ds_read_b128 v[64:67], v3 offset:10240\n"
      "v_lshrrev_b32 v109, 8, v205\n"
      "v_and_b32 v108, s44, v205\n"
      "v_and_b32 v110, s45, v205\n"
      "v_and_b32 v111, s45, v109\n"
      "v_and_b32 v109, s44, v109\n"
      "s_waitcnt lgkmcnt(3)\n"
      "v_mfma_f32_32x32x16_bf16 v[16:31], v[104:107], v[48:51], v[16:31]\n"
      "ds_read_b128 v[68:71], v3 offset:11264\n"
      "v_lshrrev_b32 v113, 8, v206\n"
      "v_and_b32 v112, s44, v206\n"
      "v_and_b32 v114, s45, v206\n"
      "v_and_b32 v115, s45, v113\n"
      "v_and_b32 v113, s44, v113\n"
      "v_mfma_f32_32x32x16_bf16 v[16:31], v[108:111], v[52:55], v[16:31]\n"
      "ds_read_b128 v[48:51], v3 offset:12288\n"
      "v_lshrrev_b32 v117, 8, v208\n"
      "v_and_b32 v116, s44, v208\n"
      "v_and_b32 v118, s45, v208\n"
      "v_and_b32 v119, s45, v117\n"
      "v_and_b32 v117, s44, v117\n"
      "s_waitcnt lgkmcnt(3)\n"
      "v_mfma_f32_32x32x16_bf16 v[16:31], v[112:115], v[56:59], v[16:31]\n"
      "ds_read_b128 v[52:55], v3 offset:13312\n"
      "v_lshrrev_b32 v121, 8, v209\n"
      "v_and_b32 v120, s44, v209\n"
      "v_and_b32 v122, s45, v209\n"
      "v_and_b32 v123, s45, v121\n"
      "v_and_b32 v121, s44, v121\n"
      "v_mfma_f32_32x32x16_bf16 v[16:31], v[116:119], v[60:63], v[16:31]\n"
      "ds_read_b128 v[56:59], v3 offset:14336\n"
      "v_lshrrev_b32 v125, 8, v210\n"
      "v_and_b32 v124, s44, v210\n"
      "v_and_b32 v126, s45, v210\n"
      "v_and_b32 v127, s45, v125\n"
      "v_and_b32 v125, s44, v125\n"
      "s_waitcnt lgkmcnt(3)\n"
      "v_mfma_f32_32x32x16_bf16 v[16:31], v[120:123], v[64:67], v[16:31]\n"
      "ds_read_b128 v[60:63], v3 offset:15360\n"
      "v_lshrrev_b32 v129, 8, v212\n"
      "v_and_b32 v128, s44, v212\n"
      "v_and_b32 v130, s45, v212\n"
      "v_and_b32 v131, s45, v129\n"
      "v_and_b32 v129, s44, v129\n"
      "v_mfma_f32_32x32x16_bf16 v[16:31], v[124:127], v[68:71], v[16:31]\n"
      "ds_read_b128 v[64:67], v3 offset:16384\n"
      "v_lshrrev_b32 v133, 8, v213\n"
      "v_and_b32 v132, s44, v213\n"
      "v_and_b32 v134, s45, v213\n"
      "v_and_b32 v135, s45, v133\n"
      "v_and_b32 v133, s44, v133\n"
      "s_waitcnt lgkmcnt(3)\n"
      "v_mfma_f32_32x32x16_bf16 v[16:31], v[128:131], v[48:51], v[16:31]\n"
      "ds_read_b128 v[68:71], v3 offset:17408\n"
      "v_lshrrev_b32 v137, 8, v214\n"
      "v_and_b32 v136, s44, v214\n"
      "v_and_b32 v138, s45, v214\n"
      "v_and_b32 v139, s45, v137\n"
      "v_and_b32 v137, s44, v137\n"
      "v_mfma_f32_32x32x16_bf16 v[16:31], v[132:135], v[52:55], v[16:31]\n"
      "ds_read_b128 v[48:51], v3 offset:18432\n"
      "v_lshrrev_b32 v141, 8, v216\n"
      "v_and_b32 v140, s44, v216\n"
      "v_and_b32 v142, s45, v216\n"
      "v_and_b32 v143, s45, v141\n"
      "v_and_b32 v141, s44, v141\n"
      "s_waitcnt lgkmcnt(3)\n"
      "v_mfma_f32_32x32x16_bf16 v[16:31], v[136:139], v[56:59], v[16:31]\n"
      "ds_read_b128 v[52:55], v3 offset:19456\n"
      "v_lshrrev_b32 v145, 8, v217\n"
      "v_and_b32 v144, s44, v217\n"
      "v_and_b32 v146, s45, v217\n"
      "v_and_b32 v147, s45, v145\n"
      "v_and_b32 v145, s44, v145\n"
      "v_mfma_f32_32x32x16_bf16 v[16:31], v[140:143], v[60:63], v[16:31]\n"
      "ds_read_b128 v[56:59], v3 offset:20480\n"
      "v_lshrrev_b32 v149, 8, v218\n"
      "v_and_b32 v148, s44, v218\n"
      "v_and_b32 v150, s45, v218\n"
      "v_and_b32 v151, s45, v149\n"
      "v_and_b32 v149, s44, v149\n"
      "s_waitcnt lgkmcnt(3)\n"
      "v_mfma_f32_32x32x16_bf16 v[16:31], v[144:147], v[64:67], v[16:31]\n"
      "ds_read_b128 v[60:63], v3 offset:21504\n"
      "s_waitcnt vmcnt(4)\n"
      ".Lq_q2:\n"
      "v_bitop3_b32 v4, v223, v227, s36 bitop3:0x7e\n"
      "v_bitop3_b32 v4, v4, v231, s36 bitop3:0xf6\n"
      "v_bitop3_b32 v4, v4, v235, s36 bitop3:0xf6\n"
      "v_bitop3_b32 v4, v4, v239, s36 bitop3:0xf6\n"
      "v_cmp_ne_u32 vcc, 0, v4\n"
      "s_cbranch_vccnz .Lq_q2r\n"
      ".Lq_q2x:\n"
      "v_lshrrev_b32 v153, 8, v220\n"
      "v_and_b32 v152, s44, v220\n"
      "v_and_b32 v154, s45, v220\n"
      "v_and_b32 v155, s45, v153\n"
      "v_and_b32 v153, s44, v153\n"
      "v_mfma_f32_32x32x16_bf16 v[16:31], v[148:151], v[68:71], v[16:31]\n"
      "ds_read_b128 v[64:67], v3 offset:22528\n"
      "v_lshrrev_b32 v157, 8, v221\n"
      "v_and_b32 v156, s44, v221\n"
      "v_and_b32 v158, s45, v221\n"
      "v_and_b32 v159, s45, v157\n"
      "v_and_b32 v157, s44, v157\n"
      "s_waitcnt lgkmcnt(3)\n"
      "v_mfma_f32_32x32x16_bf16 v[16:31], v[152:155], v[48:51], v[16:31]\n"
      "ds_read_b128 v[68:71], v3 offset:23552\n"
      "v_lshrrev_b32 v161, 8, v222\n"
      "v_and_b32 v160, s44, v222\n"
      "v_and_b32 v162, s45, v222\n"
      "v_and_b32 v163, s45, v161\n"
      "v_and_b32 v161, s44, v161\n"
      "v_mfma_f32_32x32x16_bf16 v[16:31], v[156:159], v[52:55], v[16:31]\n"
      "ds_read_b128 v[48:51], v3 offset:24576\n"
      "v_lshrrev_b32 v165, 8, v224\n"
      "v_and_b32 v164, s44, v224\n"
      "v_and_b32 v166, s45, v224\n"
      "v_and_b32 v167, s45, v165\n"
      "v_and_b32 v165, s44, v165\n"
      "s_waitcnt lgkmcnt(3)\n"
      "v_mfma_f32_32x32x16_bf16 v[16:31], v[160:163], v[56:59], v[16:31]\n"
      "ds_read_b128 v[52:55], v3 offset:25600\n"
      "v_lshrrev_b32 v169, 8, v225\n"
      "v_and_b32 v168, s44, v225\n"
      "v_and_b32 v170, s45, v225\n"
      "v_and_b32 v171, s45, v169\n"
      "v_and_b32 v169, s44, v169\n"
      "v_mfma_f32_32x32x16_bf16 v[16:31], v[164:167], v[60:63], v[16:31]\n"
      "ds_read_b128 v[56:59], v3 offset:26624\n"
      "v_lshrrev_b32 v173, 8, v226\n"
      "v_and_b32 v172, s44, v226\n"
      "v_and_b32 v174, s45, v226\n"
      "v_and_b32 v175, s45, v173\n"
      "v_and_b32 v173, s44, v173\n"
      "s_waitcnt lgkmcnt(3)\n"
      "v_mfma_f32_32x32x16_bf16 v[16:31], v[168:171], v[64:67], v[16:31]\n"
      "ds_read_b128 v[60:63], v3 offset:27648\n"
      "v_lshrrev_b32 v177, 8, v228\n"
      "v_and_b32 v176, s44, v228\n"
      "v_and_b32 v178, s45, v228\n"
      "v_and_b32 v179, s45, v177\n"
      "v_and_b32 v177, s44, v177\n"
      "v_mfma_f32_32x32x16_bf16 v[16:31], v[172:175], v[68:71], v[16:31]\n"
      "ds_read_b128 v[64:67], v3 offset:28672\n"
      "v_lshrrev_b32 v181, 8, v229\n"
      "v_and_b32 v180, s44, v229\n"
      "v_and_b32 v182, s45, v229\n"
      "v_and_b32 v183, s45, v181\n"
      "v_and_b32 v181, s44, v181\n"
      "s_waitcnt lgkmcnt(3)\n"
      "v_mfma_f32_32x32x16_bf16 v[16:31], v[176:179], v[48:51], v[16:31]\n"
      "ds_read_b128 v[68:71], v3 offset:29696\n"
      "v_lshrrev_b32 v185, 8, v230\n"
      "v_and_b32 v184, s44, v230\n"
      "v_and_b32 v186, s45, v230\n"
      "v_and_b32 v187, s45, v185\n"
      "v_and_b32 v185, s44, v185\n"
      "v_mfma_f32_32x32x16_bf16 v[16:31], v[180:183], v[52:55], v[16:31]\n"
      "ds_read_b128 v[48:51], v3 offset:30720\n"
      "v_lshrrev_b32 v189, 8, v232\n"
      "v_and_b32 v188, s44, v232\n"
      "v_and_b32 v190, s45, v232\n"
      "v_and_b32 v191, s45, v189\n"
      "v_and_b32 v189, s44, v189\n"
      "s_waitcnt lgkmcnt(3)\n"
      "v_mfma_f32_32x32x16_bf16 v[16:31], v[184:187], v[56:59], v[16:31]\n"
      "ds_read_b128 v[52:55], v3 offset:31744\n"
      "v_lshrrev_b32 v193, 8, v233\n"
      "v_and_b32 v192, s44, v233\n"
      "v_and_b32 v194, s45, v233\n"
      "v_and_b32 v195, s45, v193\n"
      "v_and_b32 v193, s44, v193\n"
      "v_mfma_f32_32x32x16_bf16 v[16:31], v[188:191], v[60:63], v[16:31]\n"
      "ds_read_b128 v[56:59], v3 offset:32768\n"
      "v_lshrrev_b32 v197, 8, v234\n"
      "v_and_b32 v196, s44, v234\n"
      "v_and_b32 v198, s45, v234\n"
      "v_and_b32 v199, s45, v197\n"
      "v_and_b32 v197, s44, v197\n"
      "s_waitcnt lgkmcnt(3)\n"
      "v_mfma_f32_32x32x16_bf16 v[16:31], v[192:195], v[64:67], v[16:31]\n"
      "ds_read_b128 v[60:63], v3 offset:33792\n"
      "v_lshrrev_b32 v201, 8, v236\n"
      "v_and_b32 v200, s44, v236\n"
      "v_and_b32 v202, s45, v236\n"
      "v_and_b32 v203, s45, v201\n"
      "v_and_b32 v201, s44, v201\n"
      "v_mfma_f32_32x32x16_bf16 v[16:31], v[196:199], v[68:71], v[16:31]\n"
      "ds_read_b128 v[64:67], v3 offset:34816\n"
      "v_lshrrev_b32 v205, 8, v237\n"
      "v_and_b32 v204, s44, v237\n"
      "v_and_b32 v206, s45, v237\n"
      "v_and_b32 v207, s45, v205\n"
      "v_and_b32 v205, s44, v205\n"
      "s_waitcnt lgkmcnt(3)\n"
      "v_mfma_f32_32x32x16_bf16 v[16:31], v[200:203], v[48:51], v[16:31]\n"
      "ds_read_b128 v[68:71], v3 offset:35840\n"
      "v_lshrrev_b32 v209, 8, v238\n"
      "v_and_b32 v208, s44, v238\n"
      "v_and_b32 v210, s45, v238\n"
      "v_and_b32 v211, s45, v209\n"
      "v_and_b32 v209, s44, v209\n"
      "v_mfma_f32_32x32x16_bf16 v[16:31], v[204:207], v[52:55], v[16:31]\n"
      "ds_read_b128 v[48:51], v3 offset:36864\n"
      "s_waitcnt vmcnt(0)\n"
      ".Lq_q3:\n"
      "v_bitop3_b32 v4, v243, v247, s36 bitop3:0x7e\n"
      "v_bitop3_b32 v4, v4, v251, s36 bitop3:0xf6\n"
      "v_bitop3_b32 v4, v4, v255, s36 bitop3:0xf6\n"
      "v_cmp_ne_u32 vcc, 0, v4\n"
      "s_cbranch_vccnz .Lq_q3r\n"
      ".Lq_q3x:\n"
      "v_lshrrev_b32 v213, 8, v240\n"
      "v_and_b32 v212, s44, v240\n"
      "v_and_b32 v214, s45, v240\n"
      "v_and_b32 v215, s45, v213\n"
      "v_and_b32 v213, s44, v213\n"
      "s_waitcnt lgkmcnt(3)\n"
      "v_mfma_f32_32x32x16_bf16 v[16:31], v[208:211], v[56:59], v[16:31]\n"
      "ds_read_b128 v[52:55], v3 offset:37888\n"
      "v_lshrrev_b32 v217, 8, v241\n"
      "v_and_b32 v216, s44, v241\n"
      "v_and_b32 v218, s45, v241\n"
      "v_and_b32 v219, s45, v217\n"
      "v_and_b32 v217, s44, v217\n"
      "v_mfma_f32_32x32x16_bf16 v[16:31], v[212:215], v[60:63], v[16:31]\n"
      "ds_read_b128 v[56:59], v3 offset:38912\n"
      "v_lshrrev_b32 v221, 8, v242\n"
      "v_and_b32 v220, s44, v242\n"
      "v_and_b32 v222, s45, v242\n"
      "v_and_b32 v223, s45, v221\n"
      "v_and_b32 v221, s44, v221\n"
      "s_waitcnt lgkmcnt(3)\n"
      "v_mfma_f32_32x32x16_bf16 v[16:31], v[216:219], v[64:67], v[16:31]\n"
      "ds_read_b128 v[60:63], v3 offset:39936\n"
      "v_lshrrev_b32 v225, 8, v244\n"
      "v_and_b32 v224, s44, v244\n"
      "v_and_b32 v226, s45, v244\n"
      "v_and_b32 v227, s45, v225\n"
      "v_and_b32 v225, s44, v225\n"
      "v_mfma_f32_32x32x16_bf16 v[16:31], v[220:223], v[68:71], v[16:31]\n"
      "ds_read_b128 v[64:67], v3 offset:40960\n"
      "v_lshrrev_b32 v229, 8, v245\n"
      "v_and_b32 v228, s44, v245\n"
      "v_and_b32 v230, s45, v245\n"
      "v_and_b32 v231, s45, v229\n"
      "v_and_b32 v229, s44, v229\n"
      "s_waitcnt lgkmcnt(3)\n"
      "v_mfma_f32_32x32x16_bf16 v[16:31], v[224:227], v[48:51], v[16:31]\n"
      "ds_read_b128 v[68:71], v3 offset:41984\n"
      "v_lshrrev_b32 v233, 8, v246\n"
      "v_and_b32 v232, s44, v246\n"
      "v_and_b32 v234, s45, v246\n"
      "v_and_b32 v235, s45, v233\n"
      "v_and_b32 v233, s44, v233\n"
      "v_mfma_f32_32x32x16_bf16 v[16:31], v[228:231], v[52:55], v[16:31]\n"
      "ds_read_b128 v[48:51], v3 offset:43008\n"
      "v_lshrrev_b32 v237, 8, v248\n"
      "v_and_b32 v236, s44, v248\n"
      "v_and_b32 v238, s45, v248\n"
      "v_and_b32 v239, s45, v237\n"
      "v_and_b32 v237, s44, v237\n"
      "s_waitcnt lgkmcnt(3)\n"
      "v_mfma_f32_32x32x16_bf16 v[16:31], v[232:235], v[56:59], v[16:31]\n"
      "ds_read_b128 v[52:55], v2 offset:0\n"
      "v_lshrrev_b32 v241, 8, v249\n"
      "v_and_b32 v240, s44, v249\n"
      "v_and_b32 v242, s45, v249\n"
      "v_and_b32 v243, s45, v241\n"
      "v_and_b32 v241, s44, v241\n"
      "v_mfma_f32_32x32x16_bf16 v[16:31], v[236:239], v[60:63], v[16:31]\n"
      "ds_read_b128 v[56:59], v2 offset:1024\n"
      "v_lshrrev_b32 v245, 8, v250\n"
      "v_and_b32 v244, s44, v250\n"
      "v_and_b32 v246, s45, v250\n"
      "v_and_b32 v247, s45, v245\n"
      "v_and_b32 v245, s44, v245\n"
      "s_waitcnt lgkmcnt(3)\n"
      "v_mfma_f32_32x32x16_bf16 v[16:31], v[240:243], v[64:67], v[16:31]\n"
      "ds_read_b128 v[60:63], v2 offset:2048\n"
      "v_lshrrev_b32 v249, 8, v252\n"
      "v_and_b32 v248, s44, v252\n"
      "v_and_b32 v250, s45, v252\n"
      "v_and_b32 v251, s45, v249\n"
      "v_and_b32 v249, s44, v249\n"
      "v_mfma_f32_32x32x16_bf16 v[16:31], v[244:247], v[68:71], v[16:31]\n"
      "ds_read_b128 v[64:67], v2 offset:3072\n"
      "ds_read_b128 v[72:75], v14 offset:1024\n"
      "ds_read_b128 v[76:79], v14 offset:1040\n"
      "s_waitcnt lgkmcnt(5)\n"
      "v_mfma_f32_32x32x16_bf16 v[16:31], v[248:251], v[48:51], v[16:31]\n"
      "ds_read_b128 v[68:71], v2 offset:4096\n"
      "v_mfma_f32_32x32x16_bf16 v[32:47], v[80:83], v[52:55], 0\n"
      "ds_read_b128 v[48:51], v2 offset:5120\n"
      "s_waitcnt lgkmcnt(5)\n"
      "v_mfma_f32_32x32x16_bf16 v[32:47], v[84:87], v[56:59], v[32:47]\n"
      "ds_read_b128 v[52:55], v2 offset:6144\n"
      "s_waitcnt lgkmcnt(3)\n"
      "v_pk_add_f32 v[72:73], v[72:73], v[74:75]\n"
      "v_pk_add_f32 v[76:77], v[76:77], v[78:79]\n"
      "v_pk_add_f32 v[72:73], v[72:73], v[76:77]\n"
      "v_add_f32 v72, v72, v73\n"
      "v_mfma_f32_32x32x16_bf16 v[32:47], v[88:91], v[60:63], v[32:47]\n"
      "ds_read_b128 v[56:59], v2 offset:7168\n"
      "v_pk_fma_f32 v[16:17], v[16:17], s[46:47], v[72:73] op_sel_hi:[1,0,0]\n"
      "v_pk_fma_f32 v[18:19], v[18:19], s[46:47], v[72:73] op_sel_hi:[1,0,0]\n"
      "v_pk_fma_f32 v[20:21], v[20:21], s[46:47], v[72:73] op_sel_hi:[1,0,0]\n"
      "v_cvt_pk_bf16_f32 v4, v16, v17\n"
      "v_cvt_pk_bf16_f32 v5, v18, v19\n"
      "v_cvt_pk_bf16_f32 v6, v20, v21\n"
      "buffer_store_dwordx4 v[4:7], v1, s[24:27], s50 offen offset:0 sc1\n"
      "v_mfma_f32_32x32x16_bf16 v[32:47], v[92:95], v[64:67], v[32:47]\n"
      "ds_read_b128 v[60:63], v2 offset:8192\n"
      "v_pk_fma_f32 v[22:23], v[22:23], s[46:47], v[72:73] op_sel_hi:[1,0,0]\n"
      "v_pk_fma_f32 v[24:25], v[24:25], s[46:47], v[72:73] op_sel_hi:[1,0,0]\n"
      "v_pk_fma_f32 v[26:27], v[26:27], s[46:47], v[72:73] op_sel_hi:[1,0,0]\n"
      "v_cvt_pk_bf16_f32 v8, v22, v23\n"
      "v_cvt_pk_bf16_f32 v9, v24, v25\n"
      "v_cvt_pk_bf16_f32 v10, v26, v27\n"
      "buffer_store_dwordx4 v[8:11], v1, s[24:27], s50 offen offset:1024 sc1\n"
      "s_waitcnt lgkmcnt(3)\n"
      "v_mfma_f32_32x32x16_bf16 v[32:47], v[96:99], v[68:71], v[32:47]\n"
      "ds_read_b128 v[64:67], v2 offset:9216\n"
      "v_pk_fma_f32 v[28:29], v[28:29], s[46:47], v[72:73] op_sel_hi:[1,0,0]\n"
      "v_pk_fma_f32 v[30:31], v[30:31], s[46:47], v[72:73] op_sel_hi:[1,0,0]\n"
      "v_cvt_pk_bf16_f32 v12, v28, v29\n"
      "v_cvt_pk_bf16_f32 v13, v30, v31\n"
      "buffer_store_dwordx4 v[12:15], v1, s[24:27], s50 offen offset:2048 sc1\n"
      "v_mfma_f32_32x32x16_bf16 v[32:47], v[100:103], v[48:51], v[32:47]\n"
      "ds_read_b128 v[68:71], v2 offset:10240\n"
      "s_waitcnt lgkmcnt(3)\n"
      "v_mfma_f32_32x32x16_bf16 v[32:47], v[104:107], v[52:55], v[32:47]\n"
      "ds_read_b128 v[48:51], v2 offset:11264\n"
      "ds_read_b128 v[72:75], v14\n"
      "ds_read_b128 v[76:79], v14 offset:16\n"
      "v_mfma_f32_32x32x16_bf16 v[32:47], v[108:111], v[56:59], v[32:47]\n"
      "ds_read_b128 v[52:55], v2 offset:12288\n"
      "s_waitcnt lgkmcnt(5)\n"
      "v_mfma_f32_32x32x16_bf16 v[32:47], v[112:115], v[60:63], v[32:47]\n"
      "ds_read_b128 v[56:59], v2 offset:13312\n"
      "v_mfma_f32_32x32x16_bf16 v[32:47], v[116:119], v[64:67], v[32:47]\n"
      "ds_read_b128 v[60:63], v2 offset:14336\n"
      "s_waitcnt lgkmcnt(5)\n"
      "v_mfma_f32_32x32x16_bf16 v[32:47], v[120:123], v[68:71], v[32:47]\n"
      "ds_read_b128 v[64:67], v2 offset:15360\n"
      "v_mfma_f32_32x32x16_bf16 v[32:47], v[124:127], v[48:51], v[32:47]\n"
      "ds_read_b128 v[68:71], v2 offset:16384\n"
      "s_waitcnt lgkmcnt(3)\n"
      "v_mfma_f32_32x32x16_bf16 v[32:47], v[128:131], v[52:55], v[32:47]\n"
      "ds_read_b128 v[48:51], v2 offset:17408\n"
      "v_pk_add_f32 v[72:73], v[72:73], v[74:75]\n"
      "v_pk_add_f32 v[76:77], v[76:77], v[78:79]\n"
      "v_pk_add_f32 v[72:73], v[72:73], v[76:77]\n"
      "v_add_f32 v72, v72, v73\n"
      "v_mfma_f32_32x32x16_bf16 v[32:47], v[132:135], v[56:59], v[32:47]\n"
      "ds_read_b128 v[52:55], v2 offset:18432\n"
      "s_cmp_ge_i32 s41, 22\n"
      "s_cselect_b32 s51, 0, 0\n"
      "s_add_u32 s51, s51, s41\n"
      "s_mul_i32 s0, s51, 43\n"
      "s_lshr_b32 s0, s0, 7\n"
      "s_mul_i32 s1, s0, 1664\n"
      "s_lshl_b32 s51, s51, 7\n"
      "s_add_u32 s1, s1, s51\n"
      "s_waitcnt lgkmcnt(3)\n"
      "v_mfma_f32_32x32x16_bf16 v[32:47], v[136:139], v[60:63], v[32:47]\n"
      "ds_read_b128 v[56:59], v2 offset:19456\n"
      "s_mul_i32 s51, s6, 384\n"
      "s_add_u32 s1, s1, s51\n"
      "s_add_u32 s1, s1, 5243392\n"
      "v_lshl_add_u32 v74, v0, 15, s1\n"
      "v_mfma_f32_32x32x16_bf16 v[32:47], v[140:143], v[64:67], v[32:47]\n"
      "ds_read_b128 v[60:63], v2 offset:20480\n"
      "s_waitcnt lgkmcnt(3)\n"
      "v_mfma_f32_32x32x16_bf16 v[32:47], v[144:147], v[68:71], v[32:47]\n"
      "ds_read_b128 v[64:67], v2 offset:21504\n"
      "v_mfma_f32_32x32x16_bf16 v[32:47], v[148:151], v[48:51], v[32:47]\n"
      "ds_read_b128 v[68:71], v2 offset:22528\n"
      "s_waitcnt lgkmcnt(3)\n"
      "v_mfma_f32_32x32x16_bf16 v[32:47], v[152:155], v[52:55], v[32:47]\n"
      "ds_read_b128 v[48:51], v2 offset:23552\n"
      "v_mfma_f32_32x32x16_bf16 v[32:47], v[156:159], v[56:59], v[32:47]\n"
      "ds_read_b128 v[52:55], v2 offset:24576\n"
      "s_waitcnt lgkmcnt(3)\n"
      "v_mfma_f32_32x32x16_bf16 v[32:47], v[160:163], v[60:63], v[32:47]\n"
      "ds_read_b128 v[56:59], v2 offset:25600\n"
      "v_mfma_f32_32x32x16_bf16 v[32:47], v[164:167], v[64:67], v[32:47]\n"
      "ds_read_b128 v[60:63], v2 offset:26624\n"
      "s_waitcnt lgkmcnt(3)\n"
      "v_mfma_f32_32x32x16_bf16 v[32:47], v[168:171], v[68:71], v[32:47]\n"
      "ds_read_b128 v[64:67], v2 offset:27648\n"
      "v_mfma_f32_32x32x16_bf16 v[32:47], v[172:175], v[48:51], v[32:47]\n"
      "ds_read_b128 v[68:71], v2 offset:28672\n"
      "s_waitcnt lgkmcnt(3)\n"
      "v_mfma_f32_32x32x16_bf16 v[32:47], v[176:179], v[52:55], v[32:47]\n"
      "ds_read_b128 v[48:51], v2 offset:29696\n"
      "v_mfma_f32_32x32x16_bf16 v[32:47], v[180:183], v[56:59], v[32:47]\n"
      "ds_read_b128 v[52:55], v2 offset:30720\n"
      "s_waitcnt lgkmcnt(3)\n"
      "v_mfma_f32_32x32x16_bf16 v[32:47], v[184:187], v[60:63], v[32:47]\n"
      "ds_read_b128 v[56:59], v2 offset:31744\n"
      "buffer_load_dwordx4 v[80:83], v1, s[24:27], s49 offen offset:0 sc1\n"
      "v_mfma_f32_32x32x16_bf16 v[32:47], v[188:191], v[64:67], v[32:47]\n"
      "ds_read_b128 v[60:63], v2 offset:32768\n"
      "buffer_load_dwordx4 v[84:87], v1, s[24:27], s49 offen offset:1024 sc1\n"
      "s_waitcnt lgkmcnt(3)\n"
      "v_mfma_f32_32x32x16_bf16 v[32:47], v[192:195], v[68:71], v[32:47]\n"
      "ds_read_b128 v[64:67], v2 offset:33792\n"
      "buffer_load_dwordx4 v[88:91], v1, s[24:27], s49 offen offset:2048 sc1\n"
      "v_mfma_f32_32x32x16_bf16 v[32:47], v[196:199], v[48:51], v[32:47]\n"
      "ds_read_b128 v[68:71], v2 offset:34816\n"
      "s_waitcnt lgkmcnt(3)\n"
      "v_mfma_f32_32x32x16_bf16 v[32:47], v[200:203], v[52:55], v[32:47]\n"
      "ds_read_b128 v[48:51], v2 offset:35840\n"
      "v_mfma_f32_32x32x16_bf16 v[32:47], v[204:207], v[56:59], v[32:47]\n"
      "ds_read_b128 v[52:55], v2 offset:36864\n"
      "s_waitcnt lgkmcnt(3)\n"
      "v_mfma_f32_32x32x16_bf16 v[32:47], v[208:211], v[60:63], v[32:47]\n"
      "ds_read_b128 v[56:59], v2 offset:37888\n"
      "v_mfma_f32_32x32x16_bf16 v[32:47], v[212:215], v[64:67], v[32:47]\n"
      "ds_read_b128 v[60:63], v2 offset:38912\n"
      "s_waitcnt lgkmcnt(3)\n"
      "v_mfma_f32_32x32x16_bf16 v[32:47], v[216:219], v[68:71], v[32:47]\n"
      "ds_read_b128 v[64:67], v2 offset:39936\n"
      "v_mfma_f32_32x32x16_bf16 v[32:47], v[220:223], v[48:51], v[32:47]\n"
      "ds_read_b128 v[68:71], v2 offset:40960\n"
      "s_waitcnt lgkmcnt(3)\n"
      "v_mfma_f32_32x32x16_bf16 v[32:47], v[224:227], v[52:55], v[32:47]\n"
      "ds_read_b128 v[48:51], v2 offset:41984\n"
      "v_mfma_f32_32x32x16_bf16 v[32:47], v[228:231], v[56:59], v[32:47]\n"
      "ds_read_b128 v[52:55], v2 offset:43008\n"
      "s_waitcnt lgkmcnt(3)\n"
      "v_mfma_f32_32x32x16_bf16 v[32:47], v[232:235], v[60:63], v[32:47]\n"
      "v_mfma_f32_32x32x16_bf16 v[32:47], v[236:239], v[64:67], v[32:47]\n"
      "s_waitcnt lgkmcnt(1)\n"
      "v_mfma_f32_32x32x16_bf16 v[32:47], v[240:243], v[68:71], v[32:47]\n"
      "v_mfma_f32_32x32x16_bf16 v[32:47], v[244:247], v[48:51], v[32:47]\n"
      "s_waitcnt lgkmcnt(0)\n"
      "v_mfma_f32_32x32x16_bf16 v[32:47], v[248:251], v[52:55], v[32:47]\n"
      "v_mov_b32 v2, v74\n"
      "s_mov_b64 s[38:39], 0x10000\n"
      "s_cmp_lt_i32 s41, 0\n"
      "s_cbranch_scc1 .Lq_tpub\n"
      "s_mov_b32 s38, 0x1ffff\n"
      "s_nop 7\n"
      "v_pk_fma_f32 v[32:33], v[32:33], s[46:47], v[72:73] op_sel_hi:[1,0,0]\n"
      "v_pk_fma_f32 v[34:35], v[34:35], s[46:47], v[72:73] op_sel_hi:[1,0,0]\n"
      "v_pk_fma_f32 v[36:37], v[36:37], s[46:47], v[72:73] op_sel_hi:[1,0,0]\n"
      "v_pk_fma_f32 v[38:39], v[38:39], s[46:47], v[72:73] op_sel_hi:[1,0,0]\n"
      "v_pk_fma_f32 v[40:41], v[40:41], s[46:47], v[72:73] op_sel_hi:[1,0,0]\n"
      "v_pk_fma_f32 v[42:43], v[42:43], s[46:47], v[72:73] op_sel_hi:[1,0,0]\n"
      "v_pk_fma_f32 v[44:45], v[44:45], s[46:47], v[72:73] op_sel_hi:[1,0,0]\n"
      "v_pk_fma_f32 v[46:47], v[46:47], s[46:47], v[72:73] op_sel_hi:[1,0,0]\n"
      "s_waitcnt vmcnt(0)\n"
      ".Lq_ex:\n"
      "v_bitop3_b32 v4, v83, v87, s36 bitop3:0x7e\n"
      "v_bitop3_b32 v4, v4, v91, s36 bitop3:0xf6\n"
      "v_cmp_ne_u32 vcc, 0, v4\n"
      "s_cbranch_vccnz .Lq_exr\n"
      ".Lq_exx:\n"
      "v_dot2_f32_bf16 v32, v80, s47, v32\n"
      "v_dot2_f32_bf16 v33, v80, s48, v33\n"
      "v_dot2_f32_bf16 v34, v81, s47, v34\n"
      "v_dot2_f32_bf16 v35, v81, s48, v35\n"
      "v_dot2_f32_bf16 v36, v82, s47, v36\n"
      "v_dot2_f32_bf16 v37, v82, s48, v37\n"
      "v_dot2_f32_bf16 v38, v84, s47, v38\n"
      "v_dot2_f32_bf16 v39, v84, s48, v39\n"
      "v_cvt_pk_bf16_f32 v16, v32, v33\n"
      "v_cvt_pk_bf16_f32 v17, v34, v35\n"
      "v_cvt_pk_bf16_f32 v18, v36, v37\n"
      "v_cvt_pk_bf16_f32 v19, v38, v39\n"
      "buffer_store_dwordx4 v[16:19], v1, s[24:27], s40 offen offset:0 sc1\n"
      "v_dot2_f32_bf16 v40, v85, s47, v40\n"
      "v_dot2_f32_bf16 v41, v85, s48, v41\n"
      "v_dot2_f32_bf16 v42, v86, s47, v42\n"
      "v_dot2_f32_bf16 v43, v86, s48, v43\n"
      "v_dot2_f32_bf16 v44, v88, s47, v44\n"
      "v_dot2_f32_bf16 v45, v88, s48, v45\n"
      "v_dot2_f32_bf16 v46, v89, s47, v46\n"
      "v_dot2_f32_bf16 v47, v89, s48, v47\n"
      "v_cvt_pk_bf16_f32 v20, v40, v41\n"
      "v_cvt_pk_bf16_f32 v21, v42, v43\n"
      "v_cvt_pk_bf16_f32 v22, v44, v45\n"
      "v_cvt_pk_bf16_f32 v23, v46, v47\n"
      "buffer_store_dwordx4 v[20:23], v1, s[24:27], s40 offen offset:1024 sc1\n"
      ".Lq_tpub:\n"
      "s_lshl_b32 s0, s2, 7\n"
      "s_add_u32 s0, s0, 4096\n"
      "v_mov_b32 v3, 161792\n"
      "v_mov_b32 v4, 1\n"
      "v_writelane_b32 v2, s0, 16\n"
      "v_mov_b32 v6, s36\n"
      "v_mov_b32 v7, s37\n"
      "s_waitcnt vmcnt(0)\n"
      "s_mov_b64 exec, 1\n"
      "ds_add_rtn_u32 v5, v3, v4\n"
      "s_waitcnt lgkmcnt(0)\n"
      "v_readfirstlane_b32 s0, v5\n"
      "s_cmp_lg_u32 s0, 3\n"
      "s_cbranch_scc1 .Lq_tpe\n"
      "s_mov_b64 exec, s[38:39]\n"
      "buffer_store_dwordx2 v[6:7], v2, s[24:27], 0 offen sc1\n"
      ".Lq_tpe:\n"
      "s_mov_b64 exec, -1\n"
      "s_branch .Lq_pc\n"
      ".Lq_pkend:\n"
      "s_waitcnt vmcnt(1)\n"
      "s_sleep 110\n"
      "s_cmp_lg_u32 s3, 0\n"
      "s_cbranch_scc1 .Lq_pc\n"
      "s_lshl_b32 s0, s2, 7\n"
      "s_add_u32 s0, s0, 4096\n"
      "v_mov_b32 v6, s36\n"
      "v_mov_b32 v7, s37\n"
      "s_mov_b64 exec, 1\n"
      "buffer_store_dwordx2 v[6:7], v1, s[24:27], s0 offen sc1\n"
      "s_mov_b64 exec, -1\n"
      "s_branch .Lq_pc\n"
      ".Lq_xr:\n"
      "s_lshr_b32 s41, s5, 2\n"
      "s_lshl_b32 s0, s4, 1\n"
      "s_add_u32 s41, s41, s0\n"
      "s_cmp_lt_u32 s3, 4\n"
      "s_cbranch_scc1 .Lq_xc\n"
      "s_setprio 3\n"
      "s_and_b32 s7, s3, 3\n"
      "s_lshl_b32 s0, s41, 2\n"
      "s_add_u32 s0, s0, s7\n"
      "s_lshl_b32 s0, s0, 15\n"
      "s_lshl_b32 s1, s7, 14\n"
      "s_lshl_b32 s30, s6, 8\n"
      "s_add_u32 s1, s1, s30\n"
      "s_add_u32 s43, s0, 1048576\n"
      "v_lshrrev_b32 v2, 3, v0\n"
      "v_and_b32 v4, 48, v0\n"
      "v_and_b32 v3, 0x70, v1\n"
      "v_lshl_add_u32 v2, v2, 10, s0\n"
      "v_xor_b32 v3, v3, v4\n"
      "v_or_b32 v8, v2, v3\n"
      "v_lshlrev_b32 v5, 2, v4\n"
      "v_lshl_add_u32 v4, v4, 6, s1\n"
      "v_and_b32 v3, 0xf0, v1\n"
      "v_xor_b32 v3, v3, v5\n"
      "v_or_b32 v4, v4, v3\n"
      "v_xor_b32 v5, 0x1010, v4\n"
      "v_xor_b32 v9, 0x2040, v8\n"
      "v_xor_b32 v6, 0x2020, v4\n"
      "v_xor_b32 v10, 0x4000, v8\n"
      "v_xor_b32 v7, 0x3030, v4\n"
      "v_xor_b32 v11, 0x6040, v8\n"
      "s_lshl_b32 s42, s41, 1\n"
      "s_add_u32 s42, s42, s6\n"
      "s_lshl_b32 s7, s7, 12\n"
      "s_mov_b32 s30, 2097152\n"
      "s_mov_b32 s34, 524288\n"
      "s_mov_b32 s35, 0x20000\n"
      "s_add_u32 s0, s42, 0\n"
      "s_and_b32 s0, s0, 3\n"
      "s_lshl_b32 s1, s0, 7\n"
      "s_lshl_b32 s0, s0, 16\n"
      "s_waitcnt lgkmcnt(0)\n"
      "s_mov_b64 s[28:29], s[8:9]\n"
      "s_mov_b64 s[32:33], s[16:17]\n"
      "s_add_u32 m0, s7, 49152\n"
      "s_nop 0\n"
      "buffer_load_dwordx4 v4, s[32:35], s0 offen lds\n"
      "s_add_u32 m0, s7, 32768\n"
      "s_nop 0\n"
      "buffer_load_dwordx4 v8, s[28:31], s1 offen lds\n"
      "s_add_u32 m0, s7, 50176\n"
      "s_nop 0\n"
      "buffer_load_dwordx4 v5, s[32:35], s0 offen lds\n"
      "s_add_u32 m0, s7, 33792\n"
      "s_nop 0\n"
      "buffer_load_dwordx4 v9, s[28:31], s1 offen lds\n"
      "s_add_u32 m0, s7, 51200\n"
      "s_nop 0\n"
      "buffer_load_dwordx4 v6, s[32:35], s0 offen lds\n"
      "s_add_u32 m0, s7, 34816\n"
      "s_nop 0\n"
      "buffer_load_dwordx4 v10, s[28:31], s1 offen lds\n"
      "s_add_u32 m0, s7, 52224\n"
      "s_nop 0\n"
      "buffer_load_dwordx4 v7, s[32:35], s0 offen lds\n"
      "s_add_u32 m0, s7, 35840\n"
      "s_nop 0\n"
      "buffer_load_dwordx4 v11, s[28:31], s1 offen lds\n"
      "s_setprio 2\n"
      "s_load_dwordx2 s[36:37], s[22:23], 0x0\n"
      "s_mov_b64 s[24:25], s[22:23]\n"
      "s_mov_b32 s26, 0xc00000\n"
      "s_mov_b32 s27, 0x20000\n"
      "v_mov_b64 v[12:13], -1\n"
      "v_mov_b64 v[14:15], -1\n"
      "s_lshl_b32 s0, s6, 13\n"
      "s_add_u32 s43, s43, s0\n"
      "s_add_u32 s44, s43, 4096\n"
      "s_add_u32 s0, s42, 1\n"
      "s_and_b32 s0, s0, 3\n"
      "s_lshl_b32 s1, s0, 7\n"
      "s_lshl_b32 s0, s0, 16\n"
      "s_add_u32 m0, s7, 81920\n"
      "s_nop 0\n"
      "buffer_load_dwordx4 v4, s[32:35], s0 offen lds\n"
      "s_add_u32 m0, s7, 65536\n"
      "s_nop 0\n"
      "buffer_load_dwordx4 v8, s[28:31], s1 offen lds\n"
      "s_add_u32 m0, s7, 82944\n"
      "s_nop 0\n"
      "buffer_load_dwordx4 v5, s[32:35], s0 offen lds\n"
      "s_add_u32 m0, s7, 66560\n"
      "s_nop 0\n"
      "buffer_load_dwordx4 v9, s[28:31], s1 offen lds\n"
      "s_add_u32 m0, s7, 83968\n"
      "s_nop 0\n"
      "buffer_load_dwordx4 v6, s[32:35], s0 offen lds\n"
      "s_add_u32 m0, s7, 67584\n"
      "s_nop 0\n"
      "buffer_load_dwordx4 v10, s[28:31], s1 offen lds\n"
      "s_add_u32 m0, s7, 84992\n"
      "s_nop 0\n"
      "buffer_load_dwordx4 v7, s[32:35], s0 offen lds\n"
      "s_add_u32 m0, s7, 68608\n"
      "s_nop 0\n"
      "buffer_load_dwordx4 v11, s[28:31], s1 offen lds\n"
      "s_waitcnt lgkmcnt(0)\n"
      "s_add_u32 s36, s36, 1\n"
      "s_addc_u32 s37, s37, 0\n"
      "s_lshl_b32 s0, s41, 15\n"
      "s_lshl_b32 s1, s6, 7\n"
      "s_add_u32 s0, s0, s1\n"
      "s_add_u32 s0, s0, 5242880\n"
      "v_lshl_add_u32 v2, v0, 11, s0\n"
      "s_lshl_b32 s0, s2, 7\n"
      "s_add_u32 s0, s0, 4096\n"
      "v_mov_b32 v16, s36\n"
      "v_mov_b32 v17, s37\n"
      "v_writelane_b32 v2, s0, 16\n"
      "s_add_u32 s0, s42, 2\n"
      "s_and_b32 s0, s0, 3\n"
      "s_lshl_b32 s1, s0, 7\n"
      "s_lshl_b32 s0, s0, 16\n"
      "s_waitcnt vmcnt(8)\n"
      "s_barrier\n"
      "s_add_u32 m0, s7, 114688\n"
      "s_nop 0\n"
      "buffer_load_dwordx4 v4, s[32:35], s0 offen lds\n"
      "s_add_u32 m0, s7, 98304\n"
      "s_nop 0\n"
      "buffer_load_dwordx4 v8, s[28:31], s1 offen lds\n"
      "s_add_u32 m0, s7, 115712\n"
      "s_nop 0\n"
      "buffer_load_dwordx4 v5, s[32:35], s0 offen lds\n"
      "s_add_u32 m0, s7, 99328\n"
      "s_nop 0\n"
      "buffer_load_dwordx4 v9, s[28:31], s1 offen lds\n"
      "s_add_u32 m0, s7, 116736\n"
      "s_nop 0\n"
      "buffer_load_dwordx4 v6, s[32:35], s0 offen lds\n"
      "s_add_u32 m0, s7, 100352\n"
      "s_nop 0\n"
      "buffer_load_dwordx4 v10, s[28:31], s1 offen lds\n"
      "s_add_u32 m0, s7, 117760\n"
      "s_nop 0\n"
      "buffer_load_dwordx4 v7, s[32:35], s0 offen lds\n"
      "s_add_u32 m0, s7, 101376\n"
      "s_nop 0\n"
      "buffer_load_dwordx4 v11, s[28:31], s1 offen lds\n"
      "s_add_u32 s0, s42, 3\n"
      "s_and_b32 s0, s0, 3\n"
      "s_lshl_b32 s1, s0, 7\n"
      "s_lshl_b32 s0, s0, 16\n"
      "s_waitcnt vmcnt(8)\n"
      "s_barrier\n"
      "s_add_u32 m0, s7, 16384\n"
      "s_nop 0\n"
      "buffer_load_dwordx4 v4, s[32:35], s0 offen lds\n"
      "s_add_u32 m0, s7, 0\n"
      "s_nop 0\n"
      "buffer_load_dwordx4 v8, s[28:31], s1 offen lds\n"
      "s_add_u32 m0, s7, 17408\n"
      "s_nop 0\n"
      "buffer_load_dwordx4 v5, s[32:35], s0 offen lds\n"
      "s_add_u32 m0, s7, 1024\n"
      "s_nop 0\n"
      "buffer_load_dwordx4 v9, s[28:31], s1 offen lds\n"
      "s_add_u32 m0, s7, 18432\n"
      "s_nop 0\n"
      "buffer_load_dwordx4 v6, s[32:35], s0 offen lds\n"
      "s_add_u32 m0, s7, 2048\n"
      "s_nop 0\n"
      "buffer_load_dwordx4 v10, s[28:31], s1 offen lds\n"
      "s_add_u32 m0, s7, 19456\n"
      "s_nop 0\n"
      "buffer_load_dwordx4 v7, s[32:35], s0 offen lds\n"
      "s_add_u32 m0, s7, 3072\n"
      "s_nop 0\n"
      "buffer_load_dwordx4 v11, s[28:31], s1 offen lds\n"
      "s_add_u32 s0, s42, 4\n"
      "s_and_b32 s0, s0, 3\n"
      "s_or_b32 s0, s0, 4\n"
      "s_lshl_b32 s1, s0, 7\n"
      "s_lshl_b32 s0, s0, 16\n"
      "s_waitcnt vmcnt(8)\n"
      "s_barrier\n"
      "s_add_u32 m0, s7, 49152\n"
      "s_nop 0\n"
      "buffer_load_dwordx4 v4, s[32:35], s0 offen lds\n"
      "s_add_u32 m0, s7, 32768\n"
      "s_nop 0\n"
      "buffer_load_dwordx4 v8, s[28:31], s1 offen lds\n"
      "s_add_u32 m0, s7, 50176\n"
      "s_nop 0\n"
      "buffer_load_dwordx4 v5, s[32:35], s0 offen lds\n"
      "s_add_u32 m0, s7, 33792\n"
      "s_nop 0\n"
      "buffer_load_dwordx4 v9, s[28:31], s1 offen lds\n"
      "s_add_u32 m0, s7, 51200\n"
      "s_nop 0\n"
      "buffer_load_dwordx4 v6, s[32:35], s0 offen lds\n"
      "s_add_u32 m0, s7, 34816\n"
      "s_nop 0\n"
      "buffer_load_dwordx4 v10, s[28:31], s1 offen lds\n"
      "s_add_u32 m0, s7, 52224\n"
      "s_nop 0\n"
      "buffer_load_dwordx4 v7, s[32:35], s0 offen lds\n"
      "s_add_u32 m0, s7, 35840\n"
      "s_nop 0\n"
      "buffer_load_dwordx4 v11, s[28:31], s1 offen lds\n"
      "s_add_u32 s0, s42, 5\n"
      "s_and_b32 s0, s0, 3\n"
      "s_or_b32 s0, s0, 4\n"
      "s_lshl_b32 s1, s0, 7\n"
      "s_lshl_b32 s0, s0, 16\n"
      "s_waitcnt vmcnt(8)\n"
      "s_barrier\n"
      "s_add_u32 m0, s7, 81920\n"
      "s_nop 0\n"
      "buffer_load_dwordx4 v4, s[32:35], s0 offen lds\n"
      "s_add_u32 m0, s7, 65536\n"
      "s_nop 0\n"
      "buffer_load_dwordx4 v8, s[28:31], s1 offen lds\n"
      "s_add_u32 m0, s7, 82944\n"
      "s_nop 0\n"
      "buffer_load_dwordx4 v5, s[32:35], s0 offen lds\n"
      "s_add_u32 m0, s7, 66560\n"
      "s_nop 0\n"
      "buffer_load_dwordx4 v9, s[28:31], s1 offen lds\n"
      "s_add_u32 m0, s7, 83968\n"
      "s_nop 0\n"
      "buffer_load_dwordx4 v6, s[32:35], s0 offen lds\n"
      "s_add_u32 m0, s7, 67584\n"
      "s_nop 0\n"
      "buffer_load_dwordx4 v10, s[28:31], s1 offen lds\n"
      "s_add_u32 m0, s7, 84992\n"
      "s_nop 0\n"
      "buffer_load_dwordx4 v7, s[32:35], s0 offen lds\n"
      "s_add_u32 m0, s7, 68608\n"
      "s_nop 0\n"
      "buffer_load_dwordx4 v11, s[28:31], s1 offen lds\n"
      "s_add_u32 s0, s42, 6\n"
      "s_and_b32 s0, s0, 3\n"
      "s_or_b32 s0, s0, 4\n"
      "s_lshl_b32 s1, s0, 7\n"
      "s_lshl_b32 s0, s0, 16\n"
      "s_waitcnt vmcnt(8)\n"
      "s_barrier\n"
      "s_add_u32 m0, s7, 114688\n"
      "s_nop 0\n"
      "buffer_load_dwordx4 v4, s[32:35], s0 offen lds\n"
      "s_add_u32 m0, s7, 98304\n"
      "s_nop 0\n"
      "buffer_load_dwordx4 v8, s[28:31], s1 offen lds\n"
      "s_add_u32 m0, s7, 115712\n"
      "s_nop 0\n"
      "buffer_load_dwordx4 v5, s[32:35], s0 offen lds\n"
      "s_add_u32 m0, s7, 99328\n"
      "s_nop 0\n"
      "buffer_load_dwordx4 v9, s[28:31], s1 offen lds\n"
      "s_add_u32 m0, s7, 116736\n"
      "s_nop 0\n"
      "buffer_load_dwordx4 v6, s[32:35], s0 offen lds\n"
      "s_add_u32 m0, s7, 100352\n"
      "s_nop 0\n"
      "buffer_load_dwordx4 v10, s[28:31], s1 offen lds\n"
      "s_add_u32 m0, s7, 117760\n"
      "s_nop 0\n"
      "buffer_load_dwordx4 v7, s[32:35], s0 offen lds\n"
      "s_add_u32 m0, s7, 101376\n"
      "s_nop 0\n"
      "buffer_load_dwordx4 v11, s[28:31], s1 offen lds\n"
      "s_add_u32 s0, s42, 7\n"
      "s_and_b32 s0, s0, 3\n"
      "s_or_b32 s0, s0, 4\n"
      "s_lshl_b32 s1, s0, 7\n"
      "s_lshl_b32 s0, s0, 16\n"
      "s_waitcnt vmcnt(8)\n"
      "s_barrier\n"
      "s_add_u32 m0, s7, 16384\n"
      "s_nop 0\n"
      "buffer_load_dwordx4 v4, s[32:35], s0 offen lds\n"
      "s_add_u32 m0, s7, 0\n"
      "s_nop 0\n"
      "buffer_load_dwordx4 v8, s[28:31], s1 offen lds\n"
      "s_add_u32 m0, s7, 17408\n"
      "s_nop 0\n"
      "buffer_load_dwordx4 v5, s[32:35], s0 offen lds\n"
      "s_add_u32 m0, s7, 1024\n"
      "s_nop 0\n"
      "buffer_load_dwordx4 v9, s[28:31], s1 offen lds\n"
      "s_add_u32 m0, s7, 18432\n"
      "s_nop 0\n"
      "buffer_load_dwordx4 v6, s[32:35], s0 offen lds\n"
      "s_add_u32 m0, s7, 2048\n"
      "s_nop 0\n"
      "buffer_load_dwordx4 v10, s[28:31], s1 offen lds\n"
      "s_add_u32 m0, s7, 19456\n"
      "s_nop 0\n"
      "buffer_load_dwordx4 v7, s[32:35], s0 offen lds\n"
      "s_add_u32 m0, s7, 3072\n"
      "s_nop 0\n"
      "buffer_load_dwordx4 v11, s[28:31], s1 offen lds\n"
      "s_waitcnt vmcnt(8)\n"
      "s_barrier\n"
      "s_waitcnt vmcnt(0)\n"
      "s_barrier\n"
      "s_setprio 0\n"
      "s_cmp_lg_u32 s3, 99\n"
      "s_cbranch_scc1 .Lq_pc\n"
      "s_mov_b64 exec, 0x1ffff\n"
      "buffer_store_dwordx2 v[16:17], v2, s[24:27], 0 offen sc1\n"
      "s_mov_b64 exec, -1\n"
      "s_branch .Lq_pc\n"
      ".Lq_xc:\n"
      "v_and_b32 v4, 4, v0\n"
      "v_add_u32 v4, 161792, v4\n"
      "v_mov_b32 v5, 0\n"
      "ds_write_b32 v4, v5\n"
      "s_and_b32 s42, s3, 1\n"
      "s_lshr_b32 s43, s3, 1\n"
      "v_and_b32 v4, 31, v0\n"
      "v_lshrrev_b32 v5, 5, v0\n"
      "v_bfe_u32 v6, v0, 1, 3\n"
      "v_xor_b32 v6, v6, v5\n"
      "v_lshlrev_b32 v6, 4, v6\n"
      "s_lshl_b32 s0, s43, 13\n"
      "v_lshl_add_u32 v4, v4, 7, s0\n"
      "v_or_b32 v240, v4, v6\n"
      "v_xor_b32 v241, 32, v240\n"
      "v_xor_b32 v242, 64, v240\n"
      "v_xor_b32 v243, 96, v240\n"
      "v_bfe_u32 v4, v0, 2, 2\n"
      "v_lshl_or_b32 v6, v5, 3, v4\n"
      "s_lshl_b32 s0, s42, 1\n"
      "v_xor_b32 v4, s0, v4\n"
      "v_lshlrev_b32 v6, 8, v6\n"
      "v_lshl_or_b32 v6, v4, 6, v6\n"
      "v_bfe_u32 v4, v0, 4, 1\n"
      "v_xor_b32 v4, v4, v5\n"
      "v_lshl_or_b32 v6, v4, 5, v6\n"
      "v_and_b32 v4, 3, v0\n"
      "v_lshl_or_b32 v248, v4, 3, v6\n"
      "v_xor_b32 v249, 16, v248\n"
      "v_xor_b32 v250, 64, v248\n"
      "v_xor_b32 v251, 0x50, v248\n"
      "v_add_u32 v244, 0x10000, v240\n"
      "v_add_u32 v252, 0x10000, v248\n"
      "v_add_u32 v245, 0x10000, v241\n"
      "v_add_u32 v253, 0x10000, v249\n"
      "v_add_u32 v246, 0x10000, v242\n"
      "v_add_u32 v254, 0x10000, v250\n"
      "v_add_u32 v247, 0x10000, v243\n"
      "v_add_u32 v255, 0x10000, v251\n"
      "s_waitcnt lgkmcnt(0)\n"
      "s_load_dwordx2 s[36:37], s[22:23], 0x0\n"
      "s_mov_b64 s[24:25], s[22:23]\n"
      "s_mov_b32 s26, 0xc00000\n"
      "s_mov_b32 s27, 0x20000\n"
      "s_lshl_b32 s0, s41, 1\n"
      "s_add_u32 s0, s0, s43\n"
      "s_lshl_b32 s44, s0, 16\n"
      "s_lshl_b32 s0, s6, 1\n"
      "s_add_u32 s0, s0, s42\n"
      "s_lshl_b32 s0, s0, 12\n"
      "s_add_u32 s44, s44, s0\n"
      "s_add_u32 s44, s44, 1048576\n"
      "s_add_u32 s45, s44, 32768\n"
      "s_lshl_b32 s0, s41, 15\n"
      "s_lshl_b32 s1, s6, 7\n"
      "s_add_u32 s0, s0, s1\n"
      "s_add_u32 s0, s0, 5242880\n"
      "v_lshl_add_u32 v2, v0, 11, s0\n"
      "s_mov_b64 s[38:39], 0x1ffff\n"
      "s_waitcnt lgkmcnt(0)\n"
      "s_add_u32 s36, s36, 1\n"
      "s_addc_u32 s37, s37, 0\n"
      "s_barrier\n"
      "ds_read_b64_tr_b16 v[80:81], v248 offset:49152\n"
      "ds_read_b64_tr_b16 v[82:83], v249 offset:50176\n"
      "ds_read_b128 v[84:87], v240 offset:32768\n"
      "ds_read_b128 v[92:95], v240 offset:36864\n"
      "ds_read_b64_tr_b16 v[88:89], v250 offset:49152\n"
      "ds_read_b64_tr_b16 v[90:91], v251 offset:50176\n"
      "ds_read_b64_tr_b16 v[96:97], v248 offset:53248\n"
      "ds_read_b64_tr_b16 v[98:99], v249 offset:54272\n"
      "ds_read_b128 v[100:103], v241 offset:32768\n"
      "ds_read_b128 v[108:111], v241 offset:36864\n"
      "ds_read_b64_tr_b16 v[104:105], v250 offset:53248\n"
      "ds_read_b64_tr_b16 v[106:107], v251 offset:54272\n"
      "s_waitcnt lgkmcnt(9)\n"
      "v_mfma_f32_32x32x16_bf16 v[16:31], v[80:83], v[84:87], 0\n"
      "ds_read_b64_tr_b16 v[112:113], v248 offset:57344\n"
      "ds_read_b64_tr_b16 v[114:115], v249 offset:58368\n"
      "s_waitcnt lgkmcnt(10)\n"
      "v_mfma_f32_32x32x16_bf16 v[32:47], v[80:83], v[92:95], 0\n"
      "ds_read_b128 v[116:119], v242 offset:32768\n"
      "ds_read_b128 v[124:127], v242 offset:36864\n"
      "s_waitcnt lgkmcnt(10)\n"
      "v_mfma_f32_32x32x16_bf16 v[48:63], v[88:91], v[84:87], 0\n"
      "ds_read_b64_tr_b16 v[120:121], v250 offset:57344\n"
      "ds_read_b64_tr_b16 v[122:123], v251 offset:58368\n"
      "v_mfma_f32_32x32x16_bf16 v[64:79], v[88:91], v[92:95], 0\n"
      "s_waitcnt lgkmcnt(6)\n"
      "v_mfma_f32_32x32x16_bf16 v[16:31], v[96:99], v[100:103], v[16:31]\n"
      "ds_read_b64_tr_b16 v[128:129], v248 offset:61440\n"
      "ds_read_b64_tr_b16 v[130:131], v249 offset:62464\n"
      "v_mfma_f32_32x32x16_bf16 v[32:47], v[96:99], v[108:111], v[32:47]\n"
      "ds_read_b128 v[132:135], v243 offset:32768\n"
      "ds_read_b128 v[140:143], v243 offset:36864\n"
      "v_mfma_f32_32x32x16_bf16 v[48:63], v[104:107], v[100:103], v[48:63]\n"
      "ds_read_b64_tr_b16 v[136:137], v250 offset:61440\n"
      "ds_read_b64_tr_b16 v[138:139], v251 offset:62464\n"
      "v_mfma_f32_32x32x16_bf16 v[64:79], v[104:107], v[108:111], v[64:79]\n"
      "s_waitcnt lgkmcnt(6)\n"
      "v_mfma_f32_32x32x16_bf16 v[16:31], v[112:115], v[116:119], v[16:31]\n"
      "s_barrier\n"
      "ds_read_b64_tr_b16 v[144:145], v252 offset:16384\n"
      "ds_read_b64_tr_b16 v[146:147], v253 offset:17408\n"
      "v_mfma_f32_32x32x16_bf16 v[32:47], v[112:115], v[124:127], v[32:47]\n"
      "ds_read_b128 v[148:151], v244 offset:0\n"
      "ds_read_b128 v[156:159], v244 offset:4096\n"
      "v_mfma_f32_32x32x16_bf16 v[48:63], v[120:123], v[116:119], v[48:63]\n"
      "ds_read_b64_tr_b16 v[152:153], v254 offset:16384\n"
      "ds_read_b64_tr_b16 v[154:155], v255 offset:17408\n"
      "v_mfma_f32_32x32x16_bf16 v[64:79], v[120:123], v[124:127], v[64:79]\n"
      "s_waitcnt lgkmcnt(6)\n"
      "v_mfma_f32_32x32x16_bf16 v[16:31], v[128:131], v[132:135], v[16:31]\n"
      "ds_read_b64_tr_b16 v[160:161], v252 offset:20480\n"
      "ds_read_b64_tr_b16 v[162:163], v253 offset:21504\n"
      "v_mfma_f32_32x32x16_bf16 v[32:47], v[128:131], v[140:143], v[32:47]\n"
      "ds_read_b128 v[164:167], v245 offset:0\n"
      "ds_read_b128 v[172:175], v245 offset:4096\n"
      "v_mfma_f32_32x32x16_bf16 v[48:63], v[136:139], v[132:135], v[48:63]\n"
      "ds_read_b64_tr_b16 v[168:169], v254 offset:20480\n"
      "ds_read_b64_tr_b16 v[170:171], v255 offset:21504\n"
      "v_mfma_f32_32x32x16_bf16 v[64:79], v[136:139], v[140:143], v[64:79]\n"
      "s_waitcnt lgkmcnt(6)\n"
      "v_mfma_f32_32x32x16_bf16 v[16:31], v[144:147], v[148:151], v[16:31]\n"
      "ds_read_b64_tr_b16 v[80:81], v252 offset:24576\n"
      "ds_read_b64_tr_b16 v[82:83], v253 offset:25600\n"
      "v_mfma_f32_32x32x16_bf16 v[32:47], v[144:147], v[156:159], v[32:47]\n"
      "ds_read_b128 v[84:87], v246 offset:0\n"
      "ds_read_b128 v[92:95], v246 offset:4096\n"
      "v_mfma_f32_32x32x16_bf16 v[48:63], v[152:155], v[148:151], v[48:63]\n"
      "ds_read_b64_tr_b16 v[88:89], v254 offset:24576\n"
      "ds_read_b64_tr_b16 v[90:91], v255 offset:25600\n"
      "v_mfma_f32_32x32x16_bf16 v[64:79], v[152:155], v[156:159], v[64:79]\n"
      "s_waitcnt lgkmcnt(6)\n"
      "v_mfma_f32_32x32x16_bf16 v[16:31], v[160:163], v[164:167], v[16:31]\n"
      "ds_read_b64_tr_b16 v[96:97], v252 offset:28672\n"
      "ds_read_b64_tr_b16 v[98:99], v253 offset:29696\n"
      "v_mfma_f32_32x32x16_bf16 v[32:47], v[160:163], v[172:175], v[32:47]\n"
      "ds_read_b128 v[100:103], v247 offset:0\n"
      "ds_read_b128 v[108:111], v247 offset:4096\n"
      "v_mfma_f32_32x32x16_bf16 v[48:63], v[168:171], v[164:167], v[48:63]\n"
      "ds_read_b64_tr_b16 v[104:105], v254 offset:28672\n"
      "ds_read_b64_tr_b16 v[106:107], v255 offset:29696\n"
      "v_mfma_f32_32x32x16_bf16 v[64:79], v[168:171], v[172:175], v[64:79]\n"
      "s_waitcnt lgkmcnt(6)\n"
      "v_mfma_f32_32x32x16_bf16 v[16:31], v[80:83], v[84:87], v[16:31]\n"
      "s_barrier\n"
      "ds_read_b64_tr_b16 v[112:113], v252 offset:49152\n"
      "ds_read_b64_tr_b16 v[114:115], v253 offset:50176\n"
      "v_mfma_f32_32x32x16_bf16 v[32:47], v[80:83], v[92:95], v[32:47]\n"
      "ds_read_b128 v[116:119], v244 offset:32768\n"
      "ds_read_b128 v[124:127], v244 offset:36864\n"
      "v_mfma_f32_32x32x16_bf16 v[48:63], v[88:91], v[84:87], v[48:63]\n"
      "ds_read_b64_tr_b16 v[120:121], v254 offset:49152\n"
      "ds_read_b64_tr_b16 v[122:123], v255 offset:50176\n"
      "v_mfma_f32_32x32x16_bf16 v[64:79], v[88:91], v[92:95], v[64:79]\n"
      "s_waitcnt lgkmcnt(6)\n"
      "v_mfma_f32_32x32x16_bf16 v[16:31], v[96:99], v[100:103], v[16:31]\n"
      "ds_read_b64_tr_b16 v[128:129], v252 offset:53248\n"
      "ds_read_b64_tr_b16 v[130:131], v253 offset:54272\n"
      "v_mfma_f32_32x32x16_bf16 v[32:47], v[96:99], v[108:111], v[32:47]\n"
      "ds_read_b128 v[132:135], v245 offset:32768\n"
      "ds_read_b128 v[140:143], v245 offset:36864\n"
      "v_mfma_f32_32x32x16_bf16 v[48:63], v[104:107], v[100:103], v[48:63]\n"
      "ds_read_b64_tr_b16 v[136:137], v254 offset:53248\n"
      "ds_read_b64_tr_b16 v[138:139], v255 offset:54272\n"
      "v_mfma_f32_32x32x16_bf16 v[64:79], v[104:107], v[108:111], v[64:79]\n"
      "s_waitcnt lgkmcnt(6)\n"
      "v_mfma_f32_32x32x16_bf16 v[16:31], v[112:115], v[116:119], v[16:31]\n"
      "ds_read_b64_tr_b16 v[144:145], v252 offset:57344\n"
      "ds_read_b64_tr_b16 v[146:147], v253 offset:58368\n"
      "v_mfma_f32_32x32x16_bf16 v[32:47], v[112:115], v[124:127], v[32:47]\n"
      "ds_read_b128 v[148:151], v246 offset:32768\n"
      "ds_read_b128 v[156:159], v246 offset:36864\n"
      "v_mfma_f32_32x32x16_bf16 v[48:63], v[120:123], v[116:119], v[48:63]\n"
      "ds_read_b64_tr_b16 v[152:153], v254 offset:57344\n"
      "ds_read_b64_tr_b16 v[154:155], v255 offset:58368\n"
      "v_mfma_f32_32x32x16_bf16 v[64:79], v[120:123], v[124:127], v[64:79]\n"
      "s_waitcnt lgkmcnt(6)\n"
      "v_mfma_f32_32x32x16_bf16 v[16:31], v[128:131], v[132:135], v[16:31]\n"
      "ds_read_b64_tr_b16 v[160:161], v252 offset:61440\n"
      "ds_read_b64_tr_b16 v[162:163], v253 offset:62464\n"
      "v_mfma_f32_32x32x16_bf16 v[32:47], v[128:131], v[140:143], v[32:47]\n"
      "ds_read_b128 v[164:167], v247 offset:32768\n"
      "ds_read_b128 v[172:175], v247 offset:36864\n"
      "v_mfma_f32_32x32x16_bf16 v[48:63], v[136:139], v[132:135], v[48:63]\n"
      "ds_read_b64_tr_b16 v[168:169], v254 offset:61440\n"
      "ds_read_b64_tr_b16 v[170:171], v255 offset:62464\n"
      "v_mfma_f32_32x32x16_bf16 v[64:79], v[136:139], v[140:143], v[64:79]\n"
      "s_waitcnt lgkmcnt(6)\n"
      "v_mfma_f32_32x32x16_bf16 v[16:31], v[144:147], v[148:151], v[16:31]\n"
      "s_barrier\n"
      "ds_read_b64_tr_b16 v[80:81], v248 offset:16384\n"
      "ds_read_b64_tr_b16 v[82:83], v249 offset:17408\n"
      "v_mfma_f32_32x32x16_bf16 v[32:47], v[144:147], v[156:159], v[32:47]\n"
      "ds_read_b128 v[84:87], v240 offset:0\n"
      "ds_read_b128 v[92:95], v240 offset:4096\n"
      "v_mfma_f32_32x32x16_bf16 v[48:63], v[152:155], v[148:151], v[48:63]\n"
      "ds_read_b64_tr_b16 v[88:89], v250 offset:16384\n"
      "ds_read_b64_tr_b16 v[90:91], v251 offset:17408\n"
      "v_mfma_f32_32x32x16_bf16 v[64:79], v[152:155], v[156:159], v[64:79]\n"
      "s_waitcnt lgkmcnt(6)\n"
      "v_mfma_f32_32x32x16_bf16 v[16:31], v[160:163], v[164:167], v[16:31]\n"
      "ds_read_b64_tr_b16 v[96:97], v248 offset:20480\n"
      "ds_read_b64_tr_b16 v[98:99], v249 offset:21504\n"
      "v_mfma_f32_32x32x16_bf16 v[32:47], v[160:163], v[172:175], v[32:47]\n"
      "ds_read_b128 v[100:103], v241 offset:0\n"
      "ds_read_b128 v[108:111], v241 offset:4096\n"
      "v_mfma_f32_32x32x16_bf16 v[48:63], v[168:171], v[164:167], v[48:63]\n"
      "ds_read_b64_tr_b16 v[104:105], v250 offset:20480\n"
      "ds_read_b64_tr_b16 v[106:107], v251 offset:21504\n"
      "v_mfma_f32_32x32x16_bf16 v[64:79], v[168:171], v[172:175], v[64:79]\n"
      "s_waitcnt lgkmcnt(6)\n"
      "v_mfma_f32_32x32x16_bf16 v[16:31], v[80:83], v[84:87], v[16:31]\n"
      "ds_read_b64_tr_b16 v[112:113], v248 offset:24576\n"
      "ds_read_b64_tr_b16 v[114:115], v249 offset:25600\n"
      "v_mfma_f32_32x32x16_bf16 v[32:47], v[80:83], v[92:95], v[32:47]\n"
      "ds_read_b128 v[116:119], v242 offset:0\n"
      "ds_read_b128 v[124:127], v242 offset:4096\n"
      "v_mfma_f32_32x32x16_bf16 v[48:63], v[88:91], v[84:87], v[48:63]\n"
      "ds_read_b64_tr_b16 v[120:121], v250 offset:24576\n"
      "ds_read_b64_tr_b16 v[122:123], v251 offset:25600\n"
      "v_mfma_f32_32x32x16_bf16 v[64:79], v[88:91], v[92:95], v[64:79]\n"
      "s_waitcnt lgkmcnt(6)\n"
      "v_mfma_f32_32x32x16_bf16 v[16:31], v[96:99], v[100:103], v[16:31]\n"
      "ds_read_b64_tr_b16 v[128:129], v248 offset:28672\n"
      "ds_read_b64_tr_b16 v[130:131], v249 offset:29696\n"
      "v_mfma_f32_32x32x16_bf16 v[32:47], v[96:99], v[108:111], v[32:47]\n"
      "ds_read_b128 v[132:135], v243 offset:0\n"
      "ds_read_b128 v[140:143], v243 offset:4096\n"
      "v_mfma_f32_32x32x16_bf16 v[48:63], v[104:107], v[100:103], v[48:63]\n"
      "ds_read_b64_tr_b16 v[136:137], v250 offset:28672\n"
      "ds_read_b64_tr_b16 v[138:139], v251 offset:29696\n"
      "v_mfma_f32_32x32x16_bf16 v[64:79], v[104:107], v[108:111], v[64:79]\n"
      "s_waitcnt lgkmcnt(6)\n"
      "v_mfma_f32_32x32x16_bf16 v[16:31], v[112:115], v[116:119], v[16:31]\n"
      "s_barrier\n"
      "ds_read_b64_tr_b16 v[144:145], v248 offset:49152\n"
      "ds_read_b64_tr_b16 v[146:147], v249 offset:50176\n"
      "v_mfma_f32_32x32x16_bf16 v[32:47], v[112:115], v[124:127], v[32:47]\n"
      "ds_read_b128 v[148:151], v240 offset:32768\n"
      "ds_read_b128 v[156:159], v240 offset:36864\n"
      "v_mfma_f32_32x32x16_bf16 v[48:63], v[120:123], v[116:119], v[48:63]\n"
      "ds_read_b64_tr_b16 v[152:153], v250 offset:49152\n"
      "ds_read_b64_tr_b16 v[154:155], v251 offset:50176\n"
      "v_mfma_f32_32x32x16_bf16 v[64:79], v[120:123], v[124:127], v[64:79]\n"
      "s_waitcnt lgkmcnt(6)\n"
      "v_mfma_f32_32x32x16_bf16 v[16:31], v[128:131], v[132:135], v[16:31]\n"
      "ds_read_b64_tr_b16 v[160:161], v248 offset:53248\n"
      "ds_read_b64_tr_b16 v[162:163], v249 offset:54272\n"
      "v_mfma_f32_32x32x16_bf16 v[32:47], v[128:131], v[140:143], v[32:47]\n"
      "ds_read_b128 v[164:167], v241 offset:32768\n"
      "ds_read_b128 v[172:175], v241 offset:36864\n"
      "v_mfma_f32_32x32x16_bf16 v[48:63], v[136:139], v[132:135], v[48:63]\n"
      "ds_read_b64_tr_b16 v[168:169], v250 offset:53248\n"
      "ds_read_b64_tr_b16 v[170:171], v251 offset:54272\n"
      "v_mfma_f32_32x32x16_bf16 v[64:79], v[136:139], v[140:143], v[64:79]\n"
      "s_waitcnt lgkmcnt(6)\n"
      "v_mfma_f32_32x32x16_bf16 v[16:31], v[144:147], v[148:151], v[16:31]\n"
      "ds_read_b64_tr_b16 v[80:81], v248 offset:57344\n"
      "ds_read_b64_tr_b16 v[82:83], v249 offset:58368\n"
      "v_mfma_f32_32x32x16_bf16 v[32:47], v[144:147], v[156:159], v[32:47]\n"
      "ds_read_b128 v[84:87], v242 offset:32768\n"
      "ds_read_b128 v[92:95], v242 offset:36864\n"
      "v_mfma_f32_32x32x16_bf16 v[48:63], v[152:155], v[148:151], v[48:63]\n"
      "ds_read_b64_tr_b16 v[88:89], v250 offset:57344\n"
      "ds_read_b64_tr_b16 v[90:91], v251 offset:58368\n"
      "v_mfma_f32_32x32x16_bf16 v[64:79], v[152:155], v[156:159], v[64:79]\n"
      "s_waitcnt lgkmcnt(6)\n"
      "v_mfma_f32_32x32x16_bf16 v[16:31], v[160:163], v[164:167], v[16:31]\n"
      "ds_read_b64_tr_b16 v[96:97], v248 offset:61440\n"
      "ds_read_b64_tr_b16 v[98:99], v249 offset:62464\n"
      "v_mfma_f32_32x32x16_bf16 v[32:47], v[160:163], v[172:175], v[32:47]\n"
      "ds_read_b128 v[100:103], v243 offset:32768\n"
      "ds_read_b128 v[108:111], v243 offset:36864\n"
      "v_mfma_f32_32x32x16_bf16 v[48:63], v[168:171], v[164:167], v[48:63]\n"
      "ds_read_b64_tr_b16 v[104:105], v250 offset:61440\n"
      "ds_read_b64_tr_b16 v[106:107], v251 offset:62464\n"
      "v_mfma_f32_32x32x16_bf16 v[64:79], v[168:171], v[172:175], v[64:79]\n"
      "s_waitcnt lgkmcnt(6)\n"
      "v_mfma_f32_32x32x16_bf16 v[16:31], v[80:83], v[84:87], v[16:31]\n"
      "s_barrier\n"
      "ds_read_b64_tr_b16 v[112:113], v252 offset:16384\n"
      "ds_read_b64_tr_b16 v[114:115], v253 offset:17408\n"
      "v_mfma_f32_32x32x16_bf16 v[32:47], v[80:83], v[92:95], v[32:47]\n"
      "ds_read_b128 v[116:119], v244 offset:0\n"
      "ds_read_b128 v[124:127], v244 offset:4096\n"
      "v_mfma_f32_32x32x16_bf16 v[48:63], v[88:91], v[84:87], v[48:63]\n"
      "ds_read_b64_tr_b16 v[120:121], v254 offset:16384\n"
      "ds_read_b64_tr_b16 v[122:123], v255 offset:17408\n"
      "v_mfma_f32_32x32x16_bf16 v[64:79], v[88:91], v[92:95], v[64:79]\n"
      "s_waitcnt lgkmcnt(6)\n"
      "v_mfma_f32_32x32x16_bf16 v[16:31], v[96:99], v[100:103], v[16:31]\n"
      "ds_read_b64_tr_b16 v[128:129], v252 offset:20480\n"
      "ds_read_b64_tr_b16 v[130:131], v253 offset:21504\n"
      "v_mfma_f32_32x32x16_bf16 v[32:47], v[96:99], v[108:111], v[32:47]\n"
      "ds_read_b128 v[132:135], v245 offset:0\n"
      "ds_read_b128 v[140:143], v245 offset:4096\n"
      "v_mfma_f32_32x32x16_bf16 v[48:63], v[104:107], v[100:103], v[48:63]\n"
      "ds_read_b64_tr_b16 v[136:137], v254 offset:20480\n"
      "ds_read_b64_tr_b16 v[138:139], v255 offset:21504\n"
      "v_mfma_f32_32x32x16_bf16 v[64:79], v[104:107], v[108:111], v[64:79]\n"
      "s_waitcnt lgkmcnt(6)\n"
      "v_mfma_f32_32x32x16_bf16 v[16:31], v[112:115], v[116:119], v[16:31]\n"
      "ds_read_b64_tr_b16 v[144:145], v252 offset:24576\n"
      "ds_read_b64_tr_b16 v[146:147], v253 offset:25600\n"
      "v_mfma_f32_32x32x16_bf16 v[32:47], v[112:115], v[124:127], v[32:47]\n"
      "ds_read_b128 v[148:151], v246 offset:0\n"
      "ds_read_b128 v[156:159], v246 offset:4096\n"
      "v_mfma_f32_32x32x16_bf16 v[48:63], v[120:123], v[116:119], v[48:63]\n"
      "ds_read_b64_tr_b16 v[152:153], v254 offset:24576\n"
      "ds_read_b64_tr_b16 v[154:155], v255 offset:25600\n"
      "v_mfma_f32_32x32x16_bf16 v[64:79], v[120:123], v[124:127], v[64:79]\n"
      "s_waitcnt lgkmcnt(6)\n"
      "v_mfma_f32_32x32x16_bf16 v[16:31], v[128:131], v[132:135], v[16:31]\n"
      "ds_read_b64_tr_b16 v[160:161], v252 offset:28672\n"
      "ds_read_b64_tr_b16 v[162:163], v253 offset:29696\n"
      "v_mfma_f32_32x32x16_bf16 v[32:47], v[128:131], v[140:143], v[32:47]\n"
      "ds_read_b128 v[164:167], v247 offset:0\n"
      "ds_read_b128 v[172:175], v247 offset:4096\n"
      "v_mfma_f32_32x32x16_bf16 v[48:63], v[136:139], v[132:135], v[48:63]\n"
      "ds_read_b64_tr_b16 v[168:169], v254 offset:28672\n"
      "ds_read_b64_tr_b16 v[170:171], v255 offset:29696\n"
      "v_mfma_f32_32x32x16_bf16 v[64:79], v[136:139], v[140:143], v[64:79]\n"
      "s_waitcnt lgkmcnt(6)\n"
      "v_mfma_f32_32x32x16_bf16 v[16:31], v[144:147], v[148:151], v[16:31]\n"
      "s_barrier\n"
      "ds_read_b64_tr_b16 v[80:81], v252 offset:49152\n"
      "ds_read_b64_tr_b16 v[82:83], v253 offset:50176\n"
      "v_mfma_f32_32x32x16_bf16 v[32:47], v[144:147], v[156:159], v[32:47]\n"
      "ds_read_b128 v[84:87], v244 offset:32768\n"
      "ds_read_b128 v[92:95], v244 offset:36864\n"
      "v_mfma_f32_32x32x16_bf16 v[48:63], v[152:155], v[148:151], v[48:63]\n"
      "ds_read_b64_tr_b16 v[88:89], v254 offset:49152\n"
      "ds_read_b64_tr_b16 v[90:91], v255 offset:50176\n"
      "v_mfma_f32_32x32x16_bf16 v[64:79], v[152:155], v[156:159], v[64:79]\n"
      "s_waitcnt lgkmcnt(6)\n"
      "v_mfma_f32_32x32x16_bf16 v[16:31], v[160:163], v[164:167], v[16:31]\n"
      "ds_read_b64_tr_b16 v[96:97], v252 offset:53248\n"
      "ds_read_b64_tr_b16 v[98:99], v253 offset:54272\n"
      "v_mfma_f32_32x32x16_bf16 v[32:47], v[160:163], v[172:175], v[32:47]\n"
      "ds_read_b128 v[100:103], v245 offset:32768\n"
      "ds_read_b128 v[108:111], v245 offset:36864\n"
      "v_mfma_f32_32x32x16_bf16 v[48:63], v[168:171], v[164:167], v[48:63]\n"
      "ds_read_b64_tr_b16 v[104:105], v254 offset:53248\n"
      "ds_read_b64_tr_b16 v[106:107], v255 offset:54272\n"
      "v_mfma_f32_32x32x16_bf16 v[64:79], v[168:171], v[172:175], v[64:79]\n"
      "s_waitcnt lgkmcnt(6)\n"
      "v_mfma_f32_32x32x16_bf16 v[16:31], v[80:83], v[84:87], v[16:31]\n"
      "ds_read_b64_tr_b16 v[112:113], v252 offset:57344\n"
      "ds_read_b64_tr_b16 v[114:115], v253 offset:58368\n"
      "v_mfma_f32_32x32x16_bf16 v[32:47], v[80:83], v[92:95], v[32:47]\n"
      "ds_read_b128 v[116:119], v246 offset:32768\n"
      "ds_read_b128 v[124:127], v246 offset:36864\n"
      "v_mfma_f32_32x32x16_bf16 v[48:63], v[88:91], v[84:87], v[48:63]\n"
      "ds_read_b64_tr_b16 v[120:121], v254 offset:57344\n"
      "ds_read_b64_tr_b16 v[122:123], v255 offset:58368\n"
      "v_mfma_f32_32x32x16_bf16 v[64:79], v[88:91], v[92:95], v[64:79]\n"
      "s_waitcnt lgkmcnt(6)\n"
      "v_mfma_f32_32x32x16_bf16 v[16:31], v[96:99], v[100:103], v[16:31]\n"
      "ds_read_b64_tr_b16 v[128:129], v252 offset:61440\n"
      "ds_read_b64_tr_b16 v[130:131], v253 offset:62464\n"
      "v_mfma_f32_32x32x16_bf16 v[32:47], v[96:99], v[108:111], v[32:47]\n"
      "ds_read_b128 v[132:135], v247 offset:32768\n"
      "ds_read_b128 v[140:143], v247 offset:36864\n"
      "v_mfma_f32_32x32x16_bf16 v[48:63], v[104:107], v[100:103], v[48:63]\n"
      "ds_read_b64_tr_b16 v[136:137], v254 offset:61440\n"
      "ds_read_b64_tr_b16 v[138:139], v255 offset:62464\n"
      "v_mfma_f32_32x32x16_bf16 v[64:79], v[104:107], v[108:111], v[64:79]\n"
      "s_waitcnt lgkmcnt(6)\n"
      "v_mfma_f32_32x32x16_bf16 v[16:31], v[112:115], v[116:119], v[16:31]\n"
      "s_barrier\n"
      "ds_read_b64_tr_b16 v[144:145], v248 offset:16384\n"
      "ds_read_b64_tr_b16 v[146:147], v249 offset:17408\n"
      "v_mfma_f32_32x32x16_bf16 v[32:47], v[112:115], v[124:127], v[32:47]\n"
      "ds_read_b128 v[148:151], v240 offset:0\n"
      "ds_read_b64_tr_b16 v[160:161], v248 offset:20480\n"
      "v_mfma_f32_32x32x16_bf16 v[48:63], v[120:123], v[116:119], v[48:63]\n"
      "ds_read_b64_tr_b16 v[162:163], v249 offset:21504\n"
      "ds_read_b128 v[164:167], v241 offset:0\n"
      "v_mfma_f32_32x32x16_bf16 v[64:79], v[120:123], v[124:127], v[64:79]\n"
      "ds_read_b64_tr_b16 v[80:81], v248 offset:24576\n"
      "ds_read_b64_tr_b16 v[82:83], v249 offset:25600\n"
      "s_waitcnt lgkmcnt(8)\n"
      "v_mfma_f32_32x32x16_bf16 v[16:31], v[128:131], v[132:135], v[16:31]\n"
      "ds_read_b128 v[84:87], v242 offset:0\n"
      "ds_read_b64_tr_b16 v[96:97], v248 offset:28672\n"
      "v_mfma_f32_32x32x16_bf16 v[32:47], v[128:131], v[140:143], v[32:47]\n"
      "ds_read_b64_tr_b16 v[98:99], v249 offset:29696\n"
      "ds_read_b128 v[100:103], v243 offset:0\n"
      "v_mfma_f32_32x32x16_bf16 v[48:63], v[136:139], v[132:135], v[48:63]\n"
      "ds_read_b128 v[156:159], v240 offset:4096\n"
      "ds_read_b128 v[172:175], v241 offset:4096\n"
      "v_mfma_f32_32x32x16_bf16 v[64:79], v[136:139], v[140:143], v[64:79]\n"
      "ds_read_b128 v[92:95], v242 offset:4096\n"
      "ds_read_b128 v[108:111], v243 offset:4096\n"
      "s_waitcnt lgkmcnt(13)\n"
      "v_mfma_f32_32x32x16_bf16 v[16:31], v[144:147], v[148:151], v[16:31]\n"
      "ds_read_b64_tr_b16 v[152:153], v250 offset:16384\n"
      "ds_read_b64_tr_b16 v[154:155], v251 offset:17408\n"
      "s_waitcnt lgkmcnt(12)\n"
      "v_mfma_f32_32x32x16_bf16 v[16:31], v[160:163], v[164:167], v[16:31]\n"
      "ds_read_b64_tr_b16 v[168:169], v250 offset:20480\n"
      "ds_read_b64_tr_b16 v[170:171], v251 offset:21504\n"
      "s_waitcnt lgkmcnt(11)\n"
      "v_mfma_f32_32x32x16_bf16 v[16:31], v[80:83], v[84:87], v[16:31]\n"
      "ds_read_b64_tr_b16 v[88:89], v250 offset:24576\n"
      "ds_read_b64_tr_b16 v[90:91], v251 offset:25600\n"
      "s_waitcnt lgkmcnt(10)\n"
      "v_mfma_f32_32x32x16_bf16 v[16:31], v[96:99], v[100:103], v[16:31]\n"
      "ds_read_b64_tr_b16 v[104:105], v250 offset:28672\n"
      "ds_read_b64_tr_b16 v[106:107], v251 offset:29696\n"
      "s_waitcnt lgkmcnt(11)\n"
      "v_mfma_f32_32x32x16_bf16 v[32:47], v[144:147], v[156:159], v[32:47]\n"
      "s_waitcnt lgkmcnt(10)\n"
      "v_mfma_f32_32x32x16_bf16 v[32:47], v[160:163], v[172:175], v[32:47]\n"
      "v_cvt_pk_bf16_f32 v176, v16, v17\n"
      "v_cvt_pk_bf16_f32 v177, v18, v19\n"
      "v_cvt_pk_bf16_f32 v178, v20, v21\n"
      "v_cvt_pk_bf16_f32 v179, v22, v23\n"
      "buffer_store_dwordx4 v[176:179], v1, s[24:27], s44 offen offset:0 sc1\n"
      "s_waitcnt lgkmcnt(9)\n"
      "v_mfma_f32_32x32x16_bf16 v[32:47], v[80:83], v[92:95], v[32:47]\n"
      "v_cvt_pk_bf16_f32 v180, v24, v25\n"
      "v_cvt_pk_bf16_f32 v181, v26, v27\n"
      "v_cvt_pk_bf16_f32 v182, v28, v29\n"
      "v_cvt_pk_bf16_f32 v183, v30, v31\n"
      "buffer_store_dwordx4 v[180:183], v1, s[24:27], s44 offen offset:1024 sc1\n"
      "s_waitcnt lgkmcnt(8)\n"
      "v_mfma_f32_32x32x16_bf16 v[32:47], v[96:99], v[108:111], v[32:47]\n"
      "s_waitcnt lgkmcnt(6)\n"
      "v_mfma_f32_32x32x16_bf16 v[48:63], v[152:155], v[148:151], v[48:63]\n"
      "s_waitcnt lgkmcnt(4)\n"
      "v_mfma_f32_32x32x16_bf16 v[48:63], v[168:171], v[164:167], v[48:63]\n"
      "v_cvt_pk_bf16_f32 v184, v32, v33\n"
      "v_cvt_pk_bf16_f32 v185, v34, v35\n"
      "v_cvt_pk_bf16_f32 v186, v36, v37\n"
      "v_cvt_pk_bf16_f32 v187, v38, v39\n"
      "buffer_store_dwordx4 v[184:187], v1, s[24:27], s45 offen offset:0 sc1\n"
      "s_waitcnt lgkmcnt(2)\n"
      "v_mfma_f32_32x32x16_bf16 v[48:63], v[88:91], v[84:87], v[48:63]\n"
      "v_cvt_pk_bf16_f32 v188, v40, v41\n"
      "v_cvt_pk_bf16_f32 v189, v42, v43\n"
      "v_cvt_pk_bf16_f32 v190, v44, v45\n"
      "v_cvt_pk_bf16_f32 v191, v46, v47\n"
      "buffer_store_dwordx4 v[188:191], v1, s[24:27], s45 offen offset:1024 sc1\n"
      "s_waitcnt lgkmcnt(0)\n"
      "v_mfma_f32_32x32x16_bf16 v[48:63], v[104:107], v[100:103], v[48:63]\n"
      "v_mfma_f32_32x32x16_bf16 v[64:79], v[152:155], v[156:159], v[64:79]\n"
      "v_mfma_f32_32x32x16_bf16 v[64:79], v[168:171], v[172:175], v[64:79]\n"
      "v_cvt_pk_bf16_f32 v192, v48, v49\n"
      "v_cvt_pk_bf16_f32 v193, v50, v51\n"
      "v_cvt_pk_bf16_f32 v194, v52, v53\n"
      "v_cvt_pk_bf16_f32 v195, v54, v55\n"
      "buffer_store_dwordx4 v[192:195], v1, s[24:27], s44 offen offset:2048 sc1\n"
      "v_mfma_f32_32x32x16_bf16 v[64:79], v[88:91], v[92:95], v[64:79]\n"
      "v_cvt_pk_bf16_f32 v196, v56, v57\n"
      "v_cvt_pk_bf16_f32 v197, v58, v59\n"
      "v_cvt_pk_bf16_f32 v198, v60, v61\n"
      "v_cvt_pk_bf16_f32 v199, v62, v63\n"
      "buffer_store_dwordx4 v[196:199], v1, s[24:27], s44 offen offset:3072 sc1\n"
      "v_mfma_f32_32x32x16_bf16 v[64:79], v[104:107], v[108:111], v[64:79]\n"
      "s_nop 7\n"
      "s_nop 3\n"
      "v_cvt_pk_bf16_f32 v200, v64, v65\n"
      "v_cvt_pk_bf16_f32 v201, v66, v67\n"
      "v_cvt_pk_bf16_f32 v202, v68, v69\n"
      "v_cvt_pk_bf16_f32 v203, v70, v71\n"
      "buffer_store_dwordx4 v[200:203], v1, s[24:27], s45 offen offset:2048 sc1\n"
      "v_cvt_pk_bf16_f32 v204, v72, v73\n"
      "v_cvt_pk_bf16_f32 v205, v74, v75\n"
      "v_cvt_pk_bf16_f32 v206, v76, v77\n"
      "v_cvt_pk_bf16_f32 v207, v78, v79\n"
      "buffer_store_dwordx4 v[204:207], v1, s[24:27], s45 offen offset:3072 sc1\n"
      "s_lshl_b32 s0, s2, 7\n"
      "s_add_u32 s0, s0, 4096\n"
      "v_mov_b32 v3, 161792\n"
      "v_mov_b32 v4, 1\n"
      "v_writelane_b32 v2, s0, 16\n"
      "v_mov_b32 v6, s36\n"
      "v_mov_b32 v7, s37\n"
      "s_waitcnt vmcnt(0)\n"
      "s_mov_b64 exec, 1\n"
      "ds_add_rtn_u32 v5, v3, v4\n"
      "s_waitcnt lgkmcnt(0)\n"
      "v_readfirstlane_b32 s0, v5\n"
      "s_cmp_lg_u32 s0, 3\n"
      "s_cbranch_scc1 .Lq_xpe\n"
      "s_mov_b64 exec, s[38:39]\n"
      "buffer_store_dwordx2 v[6:7], v2, s[24:27], 0 offen sc1\n"
      ".Lq_xpe:\n"
      "s_mov_b64 exec, -1\n"
      ".Lq_pc:\n"
      "s_setprio 0\n"
      "s_lshr_b32 s0, s4, 1\n"
      "s_lshl_b32 s0, s0, 2\n"
      "s_add_u32 s40, s0, s6\n"
      "s_and_b32 s0, s4, 1\n"
      "s_lshl_b32 s0, s0, 3\n"
      "s_lshr_b32 s41, s5, 2\n"
      "s_add_u32 s41, s41, s0\n"
      "s_cmp_eq_u32 s41, 15\n"
      "s_cbranch_scc1 .Lq_bump\n"
      "s_lshl_b32 s0, s40, 4\n"
      "s_add_u32 s0, s0, s41\n"
      "s_lshl_b32 s0, s0, 11\n"
      "s_add_u32 s0, s0, 5242880\n"
      "s_mul_i32 s1, s3, 384\n"
      "s_add_u32 s1, s1, 512\n"
      "s_cmp_eq_u32 s41, 14\n"
      "s_cselect_b32 s38, 1, 7\n"
      "s_cmp_lt_u32 s3, 4\n"
      "s_cselect_b32 s38, s38, 15\n"
      "s_cselect_b32 s1, s1, 0\n"
      "s_add_u32 s0, s0, s1\n"
      "s_mov_b32 s39, 0\n"
      "v_lshlrev_b32 v2, 7, v0\n"
      "s_mov_b32 s57, 0x8000\n"
      "s_mov_b64 exec, s[38:39]\n"
      "buffer_load_dwordx2 v[4:5], v2, s[24:27], s0 offen sc1\n"
      "s_mul_i32 s1, s40, 43\n"
      "s_lshr_b32 s1, s1, 7\n"
      "s_mul_i32 s1, s1, 3\n"
      "s_sub_u32 s1, s40, s1\n"
      "s_mul_i32 s7, s41, 3\n"
      "s_movk_i32 s42, 42\n"
      "s_add_u32 s44, s7, s1\n"
      "s_min_u32 s44, s44, s42\n"
      "s_add_u32 s1, s1, 1\n"
      "s_cmp_eq_u32 s1, 3\n"
      "s_cselect_b32 s1, 0, s1\n"
      "s_add_u32 s45, s7, s1\n"
      "s_min_u32 s45, s45, s42\n"
      "s_add_u32 s1, s1, 1\n"
      "s_cmp_eq_u32 s1, 3\n"
      "s_cselect_b32 s1, 0, s1\n"
      "s_add_u32 s46, s7, s1\n"
      "s_min_u32 s46, s46, s42\n"
      "s_add_u32 s1, s40, s41\n"
      "s_lshl_b32 s1, s1, 12\n"
      "s_add_u32 s48, s1, 0\n"
      "s_and_b32 s48, s48, 0x1c00\n"
      "s_add_u32 s49, s1, 1024\n"
      "s_and_b32 s49, s49, 0x1c00\n"
      "s_add_u32 s50, s1, 2048\n"
      "s_and_b32 s50, s50, 0x1c00\n"
      "s_add_u32 s51, s1, 3072\n"
      "s_and_b32 s51, s51, 0x1c00\n"
      "s_add_u32 s52, s1, 4096\n"
      "s_and_b32 s52, s52, 0x1c00\n"
      "s_add_u32 s53, s1, 5120\n"
      "s_and_b32 s53, s53, 0x1c00\n"
      "s_add_u32 s54, s1, 6144\n"
      "s_and_b32 s54, s54, 0x1c00\n"
      "s_add_u32 s55, s1, 7168\n"
      "s_and_b32 s55, s55, 0x1c00\n"
      ".Lq_poll:\n"
      "s_waitcnt vmcnt(0)\n"
      "v_cmp_ne_u64 vcc, s[36:37], v[4:5]\n"
      "s_cbranch_vccz .Lq_pollx\n"
      "s_sleep 1\n"
      "buffer_load_dwordx2 v[4:5], v2, s[24:27], s0 offen sc1\n"
      "s_sub_u32 s57, s57, 1\n"
      "s_cbranch_scc0 .Lq_poll\n"
      ".Lq_pollx:\n"
      "s_mov_b64 exec, -1\n"
      "s_cmp_ge_u32 s3, 4\n"
      "s_cbranch_scc1 .Lq_cc\n"
      "s_setprio 2\n"
      "s_lshl_b32 s0, s3, 13\n"
      "s_add_u32 s0, s0, 3211264\n"
      "v_add_u32 v2, s0, v1\n"
      "s_lshl_b32 s7, s3, 10\n"
      "s_lshl_b32 s0, s44, 15\n"
      "v_add_u32 v4, s0, v2\n"
      "s_lshl_b32 s0, s45, 15\n"
      "v_add_u32 v5, s0, v2\n"
      "s_lshl_b32 s0, s46, 15\n"
      "v_add_u32 v6, s0, v2\n"
      "s_add_u32 m0, s7, 90112\n"
      "s_nop 0\n"
      "buffer_load_dwordx4 v4, s[24:27], s48 offen sc1 lds\n"
      "s_add_u32 m0, s7, 94208\n"
      "s_nop 0\n"
      "buffer_load_dwordx4 v5, s[24:27], s48 offen sc1 lds\n"
      "s_add_u32 m0, s7, 98304\n"
      "s_nop 0\n"
      "buffer_load_dwordx4 v6, s[24:27], s48 offen sc1 lds\n"
      "s_add_u32 m0, s7, 102400\n"
      "s_nop 0\n"
      "buffer_load_dwordx4 v4, s[24:27], s49 offen sc1 lds\n"
      "s_add_u32 m0, s7, 106496\n"
      "s_nop 0\n"
      "buffer_load_dwordx4 v5, s[24:27], s49 offen sc1 lds\n"
      "s_add_u32 m0, s7, 110592\n"
      "s_nop 0\n"
      "buffer_load_dwordx4 v6, s[24:27], s49 offen sc1 lds\n"
      "s_add_u32 m0, s7, 114688\n"
      "s_nop 0\n"
      "buffer_load_dwordx4 v4, s[24:27], s50 offen sc1 lds\n"
      "s_add_u32 m0, s7, 118784\n"
      "s_nop 0\n"
      "buffer_load_dwordx4 v5, s[24:27], s50 offen sc1 lds\n"
      "s_add_u32 m0, s7, 122880\n"
      "s_nop 0\n"
      "buffer_load_dwordx4 v6, s[24:27], s50 offen sc1 lds\n"
      "s_and_b32 s0, s3, 3\n"
      "s_lshl_b32 s1, s40, 2\n"
      "s_add_u32 s1, s1, s0\n"
      "s_mul_i32 s1, s1, 88064\n"
      "s_mov_b64 s[28:29], s[20:21]\n"
      "s_mov_b32 s30, 5636096\n"
      "s_mov_b32 s31, 0x20000\n"
      "s_lshl_b32 s44, s44, 6\n"
      "s_add_u32 s44, s44, s1\n"
      "s_lshl_b32 s45, s45, 6\n"
      "s_add_u32 s45, s45, s1\n"
      "s_lshl_b32 s46, s46, 6\n"
      "s_add_u32 s46, s46, s1\n"
      "s_lshl_b32 s0, s0, 11\n"
      "s_add_u32 s0, s0, 40960\n"
      "v_lshlrev_b32 v10, 1, v0\n"
      "v_and_b32 v10, 48, v10\n"
      "v_xor_b32 v10, v10, v1\n"
      "v_add_u32 v10, s0, v10\n"
      "v_lshrrev_b32 v11, 2, v0\n"
      "v_and_b32 v12, 48, v1\n"
      "v_mul_u32_u24 v11, 2752, v11\n"
      "v_add_u32 v11, v11, v12\n"
      "v_add_u32 v12, 44032, v11\n"
      "s_waitcnt vmcnt(8)\n"
      "s_add_u32 m0, s7, 126976\n"
      "s_nop 0\n"
      "buffer_load_dwordx4 v4, s[24:27], s51 offen sc1 lds\n"
      "s_waitcnt vmcnt(8)\n"
      "s_add_u32 m0, s7, 131072\n"
      "s_nop 0\n"
      "buffer_load_dwordx4 v5, s[24:27], s51 offen sc1 lds\n"
      "s_waitcnt vmcnt(8)\n"
      "s_add_u32 m0, s7, 135168\n"
      "s_nop 0\n"
      "buffer_load_dwordx4 v6, s[24:27], s51 offen sc1 lds\n"
      "s_barrier\n"
      "s_waitcnt vmcnt(8)\n"
      "s_add_u32 m0, s7, 139264\n"
      "s_nop 0\n"
      "buffer_load_dwordx4 v4, s[24:27], s52 offen sc1 lds\n"
      "s_waitcnt vmcnt(8)\n"
      "s_add_u32 m0, s7, 143360\n"
      "s_nop 0\n"
      "buffer_load_dwordx4 v5, s[24:27], s52 offen sc1 lds\n"
      "s_waitcnt vmcnt(8)\n"
      "s_add_u32 m0, s7, 147456\n"
      "s_nop 0\n"
      "buffer_load_dwordx4 v6, s[24:27], s52 offen sc1 lds\n"
      "s_barrier\n"
      "s_waitcnt vmcnt(8)\n"
      "s_add_u32 m0, s7, 0\n"
      "s_nop 0\n"
      "buffer_load_dwordx4 v4, s[24:27], s53 offen sc1 lds\n"
      "s_waitcnt vmcnt(8)\n"
      "s_add_u32 m0, s7, 4096\n"
      "s_nop 0\n"
      "buffer_load_dwordx4 v4, s[24:27], s54 offen sc1 lds\n"
      "s_waitcnt vmcnt(8)\n"
      "s_add_u32 m0, s7, 8192\n"
      "s_nop 0\n"
      "buffer_load_dwordx4 v4, s[24:27], s55 offen sc1 lds\n"
      "s_barrier\n"
      "s_waitcnt vmcnt(8)\n"
      "s_add_u32 m0, s7, 12288\n"
      "s_nop 0\n"
      "buffer_load_dwordx4 v5, s[24:27], s53 offen sc1 lds\n"
      "s_waitcnt vmcnt(8)\n"
      "s_add_u32 m0, s7, 16384\n"
      "s_nop 0\n"
      "buffer_load_dwordx4 v5, s[24:27], s54 offen sc1 lds\n"
      "s_waitcnt vmcnt(8)\n"
      "s_add_u32 m0, s7, 20480\n"
      "s_nop 0\n"
      "buffer_load_dwordx4 v5, s[24:27], s55 offen sc1 lds\n"
      "s_barrier\n"
      "s_waitcnt vmcnt(8)\n"
      "s_add_u32 m0, s7, 24576\n"
      "s_nop 0\n"
      "buffer_load_dwordx4 v6, s[24:27], s53 offen sc1 lds\n"
      "s_waitcnt vmcnt(8)\n"
      "s_add_u32 m0, s7, 28672\n"
      "s_nop 0\n"
      "buffer_load_dwordx4 v6, s[24:27], s54 offen sc1 lds\n"
      "s_waitcnt vmcnt(8)\n"
      "s_add_u32 m0, s7, 32768\n"
      "s_nop 0\n"
      "buffer_load_dwordx4 v6, s[24:27], s55 offen sc1 lds\n"
      "s_barrier\n"
      "s_waitcnt vmcnt(0)\n"
      "s_barrier\n"
      "s_cmp_eq_u32 s44, s45\n"
      "s_barrier\n"
      "s_cbranch_scc1 .Lq_ys0\n"
      "ds_read_b128 v[104:107], v10 offset:0\n"
      "ds_read_b128 v[108:111], v10 offset:1024\n"
      "s_waitcnt lgkmcnt(1)\n"
      "buffer_store_dwordx4 v[104:107], v11, s[28:31], s44 offen sc1\n"
      "s_waitcnt lgkmcnt(0)\n"
      "buffer_store_dwordx4 v[108:111], v12, s[28:31], s44 offen sc1\n"
      ".Lq_ys0:\n"
      "s_cmp_eq_u32 s44, s45\n"
      "s_barrier\n"
      "s_cbranch_scc1 .Lq_ys1\n"
      "ds_read_b128 v[112:115], v10 offset:8192\n"
      "ds_read_b128 v[116:119], v10 offset:9216\n"
      "s_waitcnt lgkmcnt(1)\n"
      "buffer_store_dwordx4 v[112:115], v11, s[28:31], s45 offen sc1\n"
      "s_waitcnt lgkmcnt(0)\n"
      "buffer_store_dwordx4 v[116:119], v12, s[28:31], s45 offen sc1\n"
      ".Lq_ys1:\n"
      "s_waitcnt vmcnt(0)\n"
      "s_branch .Lq_fin\n"
      ".Lq_cc:\n"
      "s_lshl_b32 s7, s40, 2\n"
      "s_add_u32 s7, s7, s3\n"
      "s_sub_u32 s7, s7, 4\n"
      "s_lshl_b32 s0, s7, 15\n"
      "s_add_u32 s0, s0, 1048576\n"
      "v_add_u32 v4, s0, v1\n"
      "s_movk_i32 s57, 0x4000\n"
      "v_add_u32 v5, 0x2000, v4\n"
      "v_add_u32 v6, 0x4000, v4\n"
      "v_add_u32 v7, 0x6000, v4\n"
      "buffer_load_dwordx4 v[128:131], v4, s[24:27], s48 offen sc1\n"
      "buffer_load_dwordx4 v[132:135], v5, s[24:27], s48 offen sc1\n"
      "buffer_load_dwordx4 v[136:139], v6, s[24:27], s48 offen sc1\n"
      "buffer_load_dwordx4 v[140:143], v7, s[24:27], s48 offen sc1\n"
      "buffer_load_dwordx4 v[144:147], v4, s[24:27], s49 offen sc1\n"
      "buffer_load_dwordx4 v[148:151], v5, s[24:27], s49 offen sc1\n"
      "buffer_load_dwordx4 v[152:155], v6, s[24:27], s49 offen sc1\n"
      "buffer_load_dwordx4 v[156:159], v7, s[24:27], s49 offen sc1\n"
      "v_add_u32 v2, 90112, v1\n"
      "s_mov_b32 s56, 0\n"
      "s_and_b32 s0, s3, 3\n"
      "s_lshl_b32 s1, s40, 2\n"
      "s_add_u32 s1, s1, s0\n"
      "s_mul_i32 s1, s1, 88064\n"
      "s_mov_b64 s[28:29], s[20:21]\n"
      "s_mov_b32 s30, 5636096\n"
      "s_mov_b32 s31, 0x20000\n"
      "s_lshl_b32 s44, s44, 6\n"
      "s_add_u32 s44, s44, s1\n"
      "s_lshl_b32 s45, s45, 6\n"
      "s_add_u32 s45, s45, s1\n"
      "s_lshl_b32 s46, s46, 6\n"
      "s_add_u32 s46, s46, s1\n"
      "s_lshl_b32 s0, s0, 11\n"
      "s_add_u32 s0, s0, 40960\n"
      "v_and_b32 v8, 31, v0\n"
      "v_lshrrev_b32 v9, 5, v0\n"
      "v_bfe_u32 v10, v0, 1, 2\n"
      "v_xor_b32 v9, v9, v10\n"
      "v_lshlrev_b32 v9, 4, v9\n"
      "v_lshl_add_u32 v8, v8, 6, s0\n"
      "v_or_b32 v8, v8, v9\n"
      "v_xor_b32 v9, 32, v8\n"
      "v_lshlrev_b32 v10, 1, v0\n"
      "v_and_b32 v10, 48, v10\n"
      "v_xor_b32 v10, v10, v1\n"
      "v_add_u32 v10, s0, v10\n"
      "v_lshrrev_b32 v11, 2, v0\n"
      "v_and_b32 v12, 48, v1\n"
      "v_mul_u32_u24 v11, 2752, v11\n"
      "v_add_u32 v11, v11, v12\n"
      "v_add_u32 v12, 44032, v11\n"
      ".Lq_cl:\n"
      "s_barrier\n"
      "ds_read_b128 v[64:67], v2 offset:0\n"
      "ds_read_b128 v[68:71], v2 offset:4096\n"
      "ds_read_b128 v[72:75], v2 offset:8192\n"
      "ds_read_b128 v[76:79], v2 offset:1024\n"
      "ds_read_b128 v[80:83], v2 offset:5120\n"
      "ds_read_b128 v[84:87], v2 offset:9216\n"
      "s_waitcnt vmcnt(7)\n"
      "s_waitcnt lgkmcnt(4)\n"
      "v_mfma_f32_32x32x16_bf16 v[16:31], v[64:67], v[128:131], 0\n"
      "ds_read_b128 v[88:91], v2 offset:2048\n"
      "buffer_load_dwordx4 v[160:163], v4, s[24:27], s50 offen sc1\n"
      "v_mfma_f32_32x32x16_bf16 v[32:47], v[68:71], v[128:131], 0\n"
      "ds_read_b128 v[92:95], v2 offset:6144\n"
      "s_waitcnt lgkmcnt(4)\n"
      "v_mfma_f32_32x32x16_bf16 v[48:63], v[72:75], v[128:131], 0\n"
      "ds_read_b128 v[64:67], v2 offset:10240\n"
      "buffer_load_dwordx4 v[164:167], v5, s[24:27], s50 offen sc1\n"
      "s_waitcnt vmcnt(8)\n"
      "v_mfma_f32_32x32x16_bf16 v[16:31], v[76:79], v[132:135], v[16:31]\n"
      "ds_read_b128 v[68:71], v2 offset:3072\n"
      "s_waitcnt lgkmcnt(4)\n"
      "v_mfma_f32_32x32x16_bf16 v[32:47], v[80:83], v[132:135], v[32:47]\n"
      "ds_read_b128 v[72:75], v2 offset:7168\n"
      "buffer_load_dwordx4 v[168:171], v6, s[24:27], s50 offen sc1\n"
      "v_mfma_f32_32x32x16_bf16 v[48:63], v[84:87], v[132:135], v[48:63]\n"
      "ds_read_b128 v[76:79], v2 offset:11264\n"
      "s_waitcnt vmcnt(8)\n"
      "s_waitcnt lgkmcnt(4)\n"
      "v_mfma_f32_32x32x16_bf16 v[16:31], v[88:91], v[136:139], v[16:31]\n"
      "s_barrier\n"
      "ds_read_b128 v[80:83], v2 offset:12288\n"
      "buffer_load_dwordx4 v[172:175], v7, s[24:27], s50 offen sc1\n"
      "v_mfma_f32_32x32x16_bf16 v[32:47], v[92:95], v[136:139], v[32:47]\n"
      "ds_read_b128 v[84:87], v2 offset:16384\n"
      "s_waitcnt lgkmcnt(4)\n"
      "v_mfma_f32_32x32x16_bf16 v[48:63], v[64:67], v[136:139], v[48:63]\n"
      "ds_read_b128 v[88:91], v2 offset:20480\n"
      "buffer_load_dwordx4 v[176:179], v4, s[24:27], s51 offen sc1\n"
      "s_waitcnt vmcnt(9)\n"
      "v_mfma_f32_32x32x16_bf16 v[16:31], v[68:71], v[140:143], v[16:31]\n"
      "ds_read_b128 v[92:95], v2 offset:13312\n"
      "s_waitcnt lgkmcnt(4)\n"
      "v_mfma_f32_32x32x16_bf16 v[32:47], v[72:75], v[140:143], v[32:47]\n"
      "ds_read_b128 v[64:67], v2 offset:17408\n"
      "buffer_load_dwordx4 v[180:183], v5, s[24:27], s51 offen sc1\n"
      "v_mfma_f32_32x32x16_bf16 v[48:63], v[76:79], v[140:143], v[48:63]\n"
      "ds_read_b128 v[68:71], v2 offset:21504\n"
      "s_waitcnt vmcnt(9)\n"
      "s_waitcnt lgkmcnt(4)\n"
      "v_mfma_f32_32x32x16_bf16 v[16:31], v[80:83], v[144:147], v[16:31]\n"
      "ds_read_b128 v[72:75], v2 offset:14336\n"
      "buffer_load_dwordx4 v[184:187], v6, s[24:27], s51 offen sc1\n"
      "v_mfma_f32_32x32x16_bf16 v[32:47], v[84:87], v[144:147], v[32:47]\n"
      "ds_read_b128 v[76:79], v2 offset:18432\n"
      "s_waitcnt lgkmcnt(4)\n"
      "v_mfma_f32_32x32x16_bf16 v[48:63], v[88:91], v[144:147], v[48:63]\n"
      "ds_read_b128 v[80:83], v2 offset:22528\n"
      "buffer_load_dwordx4 v[188:191], v7, s[24:27], s51 offen sc1\n"
      "s_waitcnt vmcnt(10)\n"
      "v_mfma_f32_32x32x16_bf16 v[16:31], v[92:95], v[148:151], v[16:31]\n"
      "ds_read_b128 v[84:87], v2 offset:15360\n"
      "s_waitcnt lgkmcnt(4)\n"
      "v_mfma_f32_32x32x16_bf16 v[32:47], v[64:67], v[148:151], v[32:47]\n"
      "ds_read_b128 v[88:91], v2 offset:19456\n"
      "buffer_load_dwordx4 v[192:195], v4, s[24:27], s52 offen sc1\n"
      "v_mfma_f32_32x32x16_bf16 v[48:63], v[68:71], v[148:151], v[48:63]\n"
      "ds_read_b128 v[92:95], v2 offset:23552\n"
      "s_waitcnt vmcnt(10)\n"
      "s_waitcnt lgkmcnt(4)\n"
      "v_mfma_f32_32x32x16_bf16 v[16:31], v[72:75], v[152:155], v[16:31]\n"
      "s_barrier\n"
      "ds_read_b128 v[64:67], v2 offset:24576\n"
      "buffer_load_dwordx4 v[196:199], v5, s[24:27], s52 offen sc1\n"
      "v_mfma_f32_32x32x16_bf16 v[32:47], v[76:79], v[152:155], v[32:47]\n"
      "ds_read_b128 v[68:71], v2 offset:28672\n"
      "s_waitcnt lgkmcnt(4)\n"
      "v_mfma_f32_32x32x16_bf16 v[48:63], v[80:83], v[152:155], v[48:63]\n"
      "ds_read_b128 v[72:75], v2 offset:32768\n"
      "buffer_load_dwordx4 v[200:203], v6, s[24:27], s52 offen sc1\n"
      "s_waitcnt vmcnt(11)\n"
      "v_mfma_f32_32x32x16_bf16 v[16:31], v[84:87], v[156:159], v[16:31]\n"
      "ds_read_b128 v[76:79], v2 offset:25600\n"
      "s_waitcnt lgkmcnt(4)\n"
      "v_mfma_f32_32x32x16_bf16 v[32:47], v[88:91], v[156:159], v[32:47]\n"
      "ds_read_b128 v[80:83], v2 offset:29696\n"
      "buffer_load_dwordx4 v[204:207], v7, s[24:27], s52 offen sc1\n"
      "v_mfma_f32_32x32x16_bf16 v[48:63], v[92:95], v[156:159], v[48:63]\n"
      "ds_read_b128 v[84:87], v2 offset:33792\n"
      "s_waitcnt vmcnt(11)\n"
      "s_waitcnt lgkmcnt(4)\n"
      "v_mfma_f32_32x32x16_bf16 v[16:31], v[64:67], v[160:163], v[16:31]\n"
      "ds_read_b128 v[88:91], v2 offset:26624\n"
      "buffer_load_dwordx4 v[208:211], v4, s[24:27], s53 offen sc1\n"
      "v_mfma_f32_32x32x16_bf16 v[32:47], v[68:71], v[160:163], v[32:47]\n"
      "ds_read_b128 v[92:95], v2 offset:30720\n"
      "s_waitcnt lgkmcnt(4)\n"
      "v_mfma_f32_32x32x16_bf16 v[48:63], v[72:75], v[160:163], v[48:63]\n"
      "ds_read_b128 v[64:67], v2 offset:34816\n"
      "buffer_load_dwordx4 v[212:215], v5, s[24:27], s53 offen sc1\n"
      "s_waitcnt vmcnt(12)\n"
      "v_mfma_f32_32x32x16_bf16 v[16:31], v[76:79], v[164:167], v[16:31]\n"
      "ds_read_b128 v[68:71], v2 offset:27648\n"
      "s_waitcnt lgkmcnt(4)\n"
      "v_mfma_f32_32x32x16_bf16 v[32:47], v[80:83], v[164:167], v[32:47]\n"
      "ds_read_b128 v[72:75], v2 offset:31744\n"
      "buffer_load_dwordx4 v[216:219], v6, s[24:27], s53 offen sc1\n"
      "v_mfma_f32_32x32x16_bf16 v[48:63], v[84:87], v[164:167], v[48:63]\n"
      "ds_read_b128 v[76:79], v2 offset:35840\n"
      "s_waitcnt vmcnt(12)\n"
      "s_waitcnt lgkmcnt(4)\n"
      "v_mfma_f32_32x32x16_bf16 v[16:31], v[88:91], v[168:171], v[16:31]\n"
      "s_barrier\n"
      "ds_read_b128 v[80:83], v2 offset:36864\n"
      "buffer_load_dwordx4 v[220:223], v7, s[24:27], s53 offen sc1\n"
      "v_mfma_f32_32x32x16_bf16 v[32:47], v[92:95], v[168:171], v[32:47]\n"
      "ds_read_b128 v[84:87], v2 offset:40960\n"
      "s_waitcnt lgkmcnt(4)\n"
      "v_mfma_f32_32x32x16_bf16 v[48:63], v[64:67], v[168:171], v[48:63]\n"
      "ds_read_b128 v[88:91], v2 offset:45056\n"
      "buffer_load_dwordx4 v[224:227], v4, s[24:27], s54 offen sc1\n"
      "s_waitcnt vmcnt(13)\n"
      "v_mfma_f32_32x32x16_bf16 v[16:31], v[68:71], v[172:175], v[16:31]\n"
      "ds_read_b128 v[92:95], v2 offset:37888\n"
      "s_waitcnt lgkmcnt(4)\n"
      "v_mfma_f32_32x32x16_bf16 v[32:47], v[72:75], v[172:175], v[32:47]\n"
      "ds_read_b128 v[64:67], v2 offset:41984\n"
      "buffer_load_dwordx4 v[228:231], v5, s[24:27], s54 offen sc1\n"
      "v_mfma_f32_32x32x16_bf16 v[48:63], v[76:79], v[172:175], v[48:63]\n"
      "ds_read_b128 v[68:71], v2 offset:46080\n"
      "s_waitcnt vmcnt(13)\n"
      "s_waitcnt lgkmcnt(4)\n"
      "v_mfma_f32_32x32x16_bf16 v[16:31], v[80:83], v[176:179], v[16:31]\n"
      "ds_read_b128 v[72:75], v2 offset:38912\n"
      "buffer_load_dwordx4 v[232:235], v6, s[24:27], s54 offen sc1\n"
      "v_mfma_f32_32x32x16_bf16 v[32:47], v[84:87], v[176:179], v[32:47]\n"
      "ds_read_b128 v[76:79], v2 offset:43008\n"
      "s_waitcnt lgkmcnt(4)\n"
      "v_mfma_f32_32x32x16_bf16 v[48:63], v[88:91], v[176:179], v[48:63]\n"
      "ds_read_b128 v[80:83], v2 offset:47104\n"
      "buffer_load_dwordx4 v[236:239], v7, s[24:27], s54 offen sc1\n"
      "s_waitcnt vmcnt(14)\n"
      "v_mfma_f32_32x32x16_bf16 v[16:31], v[92:95], v[180:183], v[16:31]\n"
      "ds_read_b128 v[84:87], v2 offset:39936\n"
      "s_waitcnt lgkmcnt(4)\n"
      "v_mfma_f32_32x32x16_bf16 v[32:47], v[64:67], v[180:183], v[32:47]\n"
      "ds_read_b128 v[88:91], v2 offset:44032\n"
      "buffer_load_dwordx4 v[240:243], v4, s[24:27], s55 offen sc1\n"
      "v_mfma_f32_32x32x16_bf16 v[48:63], v[68:71], v[180:183], v[48:63]\n"
      "ds_read_b128 v[92:95], v2 offset:48128\n"
      "s_waitcnt vmcnt(14)\n"
      "s_waitcnt lgkmcnt(4)\n"
      "v_mfma_f32_32x32x16_bf16 v[16:31], v[72:75], v[184:187], v[16:31]\n"
      "s_barrier\n"
      "ds_read_b128 v[64:67], v2 offset:49152\n"
      "buffer_load_dwordx4 v[244:247], v5, s[24:27], s55 offen sc1\n"
      "v_mfma_f32_32x32x16_bf16 v[32:47], v[76:79], v[184:187], v[32:47]\n"
      "ds_read_b128 v[68:71], v2 offset:53248\n"
      "s_waitcnt lgkmcnt(4)\n"
      "v_mfma_f32_32x32x16_bf16 v[48:63], v[80:83], v[184:187], v[48:63]\n"
      "ds_read_b128 v[72:75], v2 offset:57344\n"
      "buffer_load_dwordx4 v[248:251], v6, s[24:27], s55 offen sc1\n"
      "s_waitcnt vmcnt(15)\n"
      "v_mfma_f32_32x32x16_bf16 v[16:31], v[84:87], v[188:191], v[16:31]\n"
      "ds_read_b128 v[76:79], v2 offset:50176\n"
      "s_waitcnt lgkmcnt(4)\n"
      "v_mfma_f32_32x32x16_bf16 v[32:47], v[88:91], v[188:191], v[32:47]\n"
      "ds_read_b128 v[80:83], v2 offset:54272\n"
      "buffer_load_dwordx4 v[252:255], v7, s[24:27], s55 offen sc1\n"
      "v_mfma_f32_32x32x16_bf16 v[48:63], v[92:95], v[188:191], v[48:63]\n"
      "ds_read_b128 v[84:87], v2 offset:58368\n"
      "s_waitcnt vmcnt(15)\n"
      "s_waitcnt lgkmcnt(4)\n"
      "v_mfma_f32_32x32x16_bf16 v[16:31], v[64:67], v[192:195], v[16:31]\n"
      "ds_read_b128 v[88:91], v2 offset:51200\n"
      "v_mfma_f32_32x32x16_bf16 v[32:47], v[68:71], v[192:195], v[32:47]\n"
      "ds_read_b128 v[92:95], v2 offset:55296\n"
      "s_waitcnt lgkmcnt(4)\n"
      "v_mfma_f32_32x32x16_bf16 v[48:63], v[72:75], v[192:195], v[48:63]\n"
      "ds_read_b128 v[64:67], v2 offset:59392\n"
      "s_waitcnt vmcnt(14)\n"
      "v_mfma_f32_32x32x16_bf16 v[16:31], v[76:79], v[196:199], v[16:31]\n"
      "ds_read_b128 v[68:71], v2 offset:52224\n"
      "s_waitcnt lgkmcnt(4)\n"
      "v_mfma_f32_32x32x16_bf16 v[32:47], v[80:83], v[196:199], v[32:47]\n"
      "ds_read_b128 v[72:75], v2 offset:56320\n"
      "v_mfma_f32_32x32x16_bf16 v[48:63], v[84:87], v[196:199], v[48:63]\n"
      "ds_read_b128 v[76:79], v2 offset:60416\n"
      "s_waitcnt vmcnt(13)\n"
      "s_waitcnt lgkmcnt(4)\n"
      "v_mfma_f32_32x32x16_bf16 v[16:31], v[88:91], v[200:203], v[16:31]\n"
      "s_barrier\n"
      "ds_read_b128 v[80:83], v1 offset:0\n"
      "v_mfma_f32_32x32x16_bf16 v[32:47], v[92:95], v[200:203], v[32:47]\n"
      "ds_read_b128 v[84:87], v1 offset:1024\n"
      "s_waitcnt lgkmcnt(4)\n"
      "v_mfma_f32_32x32x16_bf16 v[48:63], v[64:67], v[200:203], v[48:63]\n"
      "ds_read_b128 v[88:91], v1 offset:2048\n"
      "s_waitcnt vmcnt(12)\n"
      "v_mfma_f32_32x32x16_bf16 v[16:31], v[68:71], v[204:207], v[16:31]\n"
      "ds_read_b128 v[92:95], v1 offset:3072\n"
      "s_waitcnt lgkmcnt(4)\n"
      "v_mfma_f32_32x32x16_bf16 v[32:47], v[72:75], v[204:207], v[32:47]\n"
      "ds_read_b128 v[64:67], v1 offset:4096\n"
      "v_mfma_f32_32x32x16_bf16 v[48:63], v[76:79], v[204:207], v[48:63]\n"
      "ds_read_b128 v[68:71], v1 offset:5120\n"
      "s_waitcnt vmcnt(11)\n"
      "s_waitcnt lgkmcnt(4)\n"
      "v_mfma_f32_32x32x16_bf16 v[16:31], v[80:83], v[208:211], v[16:31]\n"
      "ds_read_b128 v[72:75], v1 offset:6144\n"
      "s_waitcnt vmcnt(10)\n"
      "v_mfma_f32_32x32x16_bf16 v[16:31], v[84:87], v[212:215], v[16:31]\n"
      "ds_read_b128 v[76:79], v1 offset:7168\n"
      "s_waitcnt vmcnt(9)\n"
      "s_waitcnt lgkmcnt(4)\n"
      "v_mfma_f32_32x32x16_bf16 v[16:31], v[88:91], v[216:219], v[16:31]\n"
      "ds_read_b128 v[80:83], v1 offset:8192\n"
      "s_waitcnt vmcnt(8)\n"
      "v_mfma_f32_32x32x16_bf16 v[16:31], v[92:95], v[220:223], v[16:31]\n"
      "ds_read_b128 v[84:87], v1 offset:9216\n"
      "s_waitcnt vmcnt(7)\n"
      "s_waitcnt lgkmcnt(4)\n"
      "v_mfma_f32_32x32x16_bf16 v[16:31], v[64:67], v[224:227], v[16:31]\n"
      "ds_read_b128 v[88:91], v1 offset:10240\n"
      "s_waitcnt vmcnt(6)\n"
      "v_mfma_f32_32x32x16_bf16 v[16:31], v[68:71], v[228:231], v[16:31]\n"
      "ds_read_b128 v[92:95], v1 offset:11264\n"
      "s_waitcnt vmcnt(5)\n"
      "s_waitcnt lgkmcnt(4)\n"
      "v_mfma_f32_32x32x16_bf16 v[16:31], v[72:75], v[232:235], v[16:31]\n"
      "ds_read_b128 v[64:67], v1 offset:12288\n"
      "s_waitcnt vmcnt(4)\n"
      "v_mfma_f32_32x32x16_bf16 v[16:31], v[76:79], v[236:239], v[16:31]\n"
      "ds_read_b128 v[68:71], v1 offset:13312\n"
      "s_waitcnt vmcnt(3)\n"
      "s_waitcnt lgkmcnt(4)\n"
      "v_mfma_f32_32x32x16_bf16 v[16:31], v[80:83], v[240:243], v[16:31]\n"
      "ds_read_b128 v[72:75], v1 offset:14336\n"
      "s_waitcnt vmcnt(2)\n"
      "v_mfma_f32_32x32x16_bf16 v[16:31], v[84:87], v[244:247], v[16:31]\n"
      "ds_read_b128 v[76:79], v1 offset:15360\n"
      "s_waitcnt vmcnt(1)\n"
      "s_waitcnt lgkmcnt(4)\n"
      "v_mfma_f32_32x32x16_bf16 v[16:31], v[88:91], v[248:251], v[16:31]\n"
      "ds_read_b128 v[80:83], v1 offset:16384\n"
      "s_waitcnt vmcnt(0)\n"
      "v_mfma_f32_32x32x16_bf16 v[16:31], v[92:95], v[252:255], v[16:31]\n"
      "ds_read_b128 v[84:87], v1 offset:17408\n"
      "s_waitcnt lgkmcnt(4)\n"
      "v_mfma_f32_32x32x16_bf16 v[32:47], v[64:67], v[208:211], v[32:47]\n"
      "ds_read_b128 v[88:91], v1 offset:18432\n"
      "v_mfma_f32_32x32x16_bf16 v[32:47], v[68:71], v[212:215], v[32:47]\n"
      "ds_read_b128 v[92:95], v1 offset:19456\n"
      "v_cvt_pk_bf16_f32 v96, v16, v17\n"
      "v_cvt_pk_bf16_f32 v97, v18, v19\n"
      "v_cvt_pk_bf16_f32 v98, v20, v21\n"
      "v_cvt_pk_bf16_f32 v99, v22, v23\n"
      "ds_write_b128 v8, v[96:99] offset:0\n"
      "s_waitcnt lgkmcnt(5)\n"
      "v_mfma_f32_32x32x16_bf16 v[32:47], v[72:75], v[216:219], v[32:47]\n"
      "ds_read_b128 v[64:67], v1 offset:20480\n"
      "v_cvt_pk_bf16_f32 v100, v24, v25\n"
      "v_cvt_pk_bf16_f32 v101, v26, v27\n"
      "v_cvt_pk_bf16_f32 v102, v28, v29\n"
      "v_cvt_pk_bf16_f32 v103, v30, v31\n"
      "ds_write_b128 v9, v[100:103] offset:0\n"
      "v_mfma_f32_32x32x16_bf16 v[32:47], v[76:79], v[220:223], v[32:47]\n"
      "ds_read_b128 v[68:71], v1 offset:21504\n"
      "s_waitcnt lgkmcnt(6)\n"
      "v_mfma_f32_32x32x16_bf16 v[32:47], v[80:83], v[224:227], v[32:47]\n"
      "ds_read_b128 v[72:75], v1 offset:22528\n"
      "v_mfma_f32_32x32x16_bf16 v[32:47], v[84:87], v[228:231], v[32:47]\n"
      "ds_read_b128 v[76:79], v1 offset:23552\n"
      "s_waitcnt lgkmcnt(3)\n"
      "s_barrier\n"
      "v_mfma_f32_32x32x16_bf16 v[32:47], v[88:91], v[232:235], v[32:47]\n"
      "ds_read_b128 v[80:83], v1 offset:24576\n"
      "v_mfma_f32_32x32x16_bf16 v[32:47], v[92:95], v[236:239], v[32:47]\n"
      "ds_read_b128 v[84:87], v1 offset:25600\n"
      "v_mfma_f32_32x32x16_bf16 v[32:47], v[64:67], v[240:243], v[32:47]\n"
      "ds_read_b128 v[88:91], v1 offset:26624\n"
      "s_waitcnt lgkmcnt(4)\n"
      "v_mfma_f32_32x32x16_bf16 v[32:47], v[68:71], v[244:247], v[32:47]\n"
      "ds_read_b128 v[92:95], v1 offset:27648\n"
      "v_mfma_f32_32x32x16_bf16 v[32:47], v[72:75], v[248:251], v[32:47]\n"
      "ds_read_b128 v[64:67], v1 offset:28672\n"
      "s_waitcnt lgkmcnt(4)\n"
      "v_mfma_f32_32x32x16_bf16 v[32:47], v[76:79], v[252:255], v[32:47]\n"
      "ds_read_b128 v[68:71], v1 offset:29696\n"
      "v_mfma_f32_32x32x16_bf16 v[48:63], v[80:83], v[208:211], v[48:63]\n"
      "ds_read_b128 v[72:75], v1 offset:30720\n"
      "s_waitcnt lgkmcnt(4)\n"
      "v_mfma_f32_32x32x16_bf16 v[48:63], v[84:87], v[212:215], v[48:63]\n"
      "ds_read_b128 v[76:79], v1 offset:31744\n"
      "v_cvt_pk_bf16_f32 v96, v32, v33\n"
      "v_cvt_pk_bf16_f32 v97, v34, v35\n"
      "v_cvt_pk_bf16_f32 v98, v36, v37\n"
      "v_cvt_pk_bf16_f32 v99, v38, v39\n"
      "ds_write_b128 v8, v[96:99] offset:8192\n"
      "v_mfma_f32_32x32x16_bf16 v[48:63], v[88:91], v[216:219], v[48:63]\n"
      "ds_read_b128 v[80:83], v1 offset:32768\n"
      "v_cvt_pk_bf16_f32 v100, v40, v41\n"
      "v_cvt_pk_bf16_f32 v101, v42, v43\n"
      "v_cvt_pk_bf16_f32 v102, v44, v45\n"
      "v_cvt_pk_bf16_f32 v103, v46, v47\n"
      "ds_write_b128 v9, v[100:103] offset:8192\n"
      "s_waitcnt lgkmcnt(6)\n"
      "v_mfma_f32_32x32x16_bf16 v[48:63], v[92:95], v[220:223], v[48:63]\n"
      "ds_read_b128 v[84:87], v1 offset:33792\n"
      "v_mfma_f32_32x32x16_bf16 v[48:63], v[64:67], v[224:227], v[48:63]\n"
      "ds_read_b128 v[88:91], v1 offset:34816\n"
      "s_waitcnt lgkmcnt(6)\n"
      "v_mfma_f32_32x32x16_bf16 v[48:63], v[68:71], v[228:231], v[48:63]\n"
      "ds_read_b128 v[92:95], v1 offset:35840\n"
      "s_waitcnt lgkmcnt(3)\n"
      "s_barrier\n"
      "v_mfma_f32_32x32x16_bf16 v[48:63], v[72:75], v[232:235], v[48:63]\n"
      "v_mfma_f32_32x32x16_bf16 v[48:63], v[76:79], v[236:239], v[48:63]\n"
      "v_mfma_f32_32x32x16_bf16 v[48:63], v[80:83], v[240:243], v[48:63]\n"
      "s_waitcnt lgkmcnt(1)\n"
      "v_mfma_f32_32x32x16_bf16 v[48:63], v[84:87], v[244:247], v[48:63]\n"
      "v_mfma_f32_32x32x16_bf16 v[48:63], v[88:91], v[248:251], v[48:63]\n"
      "s_waitcnt lgkmcnt(0)\n"
      "v_mfma_f32_32x32x16_bf16 v[48:63], v[92:95], v[252:255], v[48:63]\n"
      "s_nop 7\n"
      "s_nop 3\n"
      "v_cvt_pk_bf16_f32 v96, v48, v49\n"
      "v_cvt_pk_bf16_f32 v97, v50, v51\n"
      "v_cvt_pk_bf16_f32 v98, v52, v53\n"
      "v_cvt_pk_bf16_f32 v99, v54, v55\n"
      "ds_write_b128 v8, v[96:99] offset:16384\n"
      "v_cvt_pk_bf16_f32 v100, v56, v57\n"
      "v_cvt_pk_bf16_f32 v101, v58, v59\n"
      "v_cvt_pk_bf16_f32 v102, v60, v61\n"
      "v_cvt_pk_bf16_f32 v103, v62, v63\n"
      "ds_write_b128 v9, v[100:103] offset:16384\n"
      "ds_read_b128 v[120:123], v10 offset:16384\n"
      "ds_read_b128 v[124:127], v10 offset:17408\n"
      "s_waitcnt lgkmcnt(1)\n"
      "buffer_store_dwordx4 v[120:123], v11, s[28:31], s46 offen sc1\n"
      "s_waitcnt lgkmcnt(0)\n"
      "buffer_store_dwordx4 v[124:127], v12, s[28:31], s46 offen sc1\n"
      "s_branch .Lq_fin\n"
      ".Lq_bump:\n"
      "s_or_b32 s0, s40, s3\n"
      "s_cmp_lg_u32 s0, 0\n"
      "s_cbranch_scc1 .Lq_fin\n"
      "v_lshlrev_b32 v2, 7, v0\n"
      "s_mov_b32 s57, 0x8000\n"
      "s_movk_i32 s44, 4096\n"
      "s_movk_i32 s45, 12288\n"
      "s_movk_i32 s46, 20480\n"
      "s_movk_i32 s47, 28672\n"
      ".Lq_bl:\n"
      "s_sleep 16\n"
      "buffer_load_dwordx2 v[4:5], v2, s[24:27], s44 offen sc1\n"
      "buffer_load_dwordx2 v[6:7], v2, s[24:27], s45 offen sc1\n"
      "buffer_load_dwordx2 v[8:9], v2, s[24:27], s46 offen sc1\n"
      "buffer_load_dwordx2 v[10:11], v2, s[24:27], s47 offen sc1\n"
      "s_waitcnt vmcnt(0)\n"
      "v_cmp_ne_u64 vcc, s[36:37], v[4:5]\n"
      "v_cmp_ne_u64 s[0:1], s[36:37], v[6:7]\n"
      "v_cmp_ne_u64 s[48:49], s[36:37], v[8:9]\n"
      "v_cmp_ne_u64 s[50:51], s[36:37], v[10:11]\n"
      "s_or_b64 s[0:1], s[0:1], vcc\n"
      "s_or_b64 s[48:49], s[48:49], s[50:51]\n"
      "s_or_b64 s[0:1], s[0:1], s[48:49]\n"
      "s_cbranch_scc0 .Lq_blx\n"
      "s_sub_u32 s57, s57, 1\n"
      "s_cbranch_scc0 .Lq_bl\n"
      ".Lq_blx:\n"
      "v_mov_b32 v4, s36\n"
      "v_mov_b32 v5, s37\n"
      "s_mov_b64 exec, 1\n"
      "buffer_store_dwordx2 v[4:5], v2, s[24:27], 0 offen sc1\n"
      "s_mov_b64 exec, -1\n"
      "s_branch .Lq_fin\n"
      ".Lq_q0r:\n"
      "buffer_load_dwordx4 v[196:199], v1, s[24:27], s53 offen offset:0 sc1\n"
      "buffer_load_dwordx4 v[200:203], v1, s[24:27], s53 offen offset:1024 sc1\n"
      "s_waitcnt vmcnt(0)\n"
      "s_sub_u32 s57, s57, 1\n"
      "s_cbranch_scc0 .Lq_q0\n"
      "s_mov_b32 s57, 0\n"
      "s_branch .Lq_q0x\n"
      ".Lq_q1r:\n"
      "buffer_load_dwordx4 v[204:207], v1, s[24:27], s53 offen offset:2048 sc1\n"
      "buffer_load_dwordx4 v[208:211], v1, s[24:27], s53 offen offset:3072 sc1\n"
      "buffer_load_dwordx4 v[212:215], v1, s[24:27], s54 offen offset:0 sc1\n"
      "buffer_load_dwordx4 v[216:219], v1, s[24:27], s54 offen offset:1024 sc1\n"
      "s_waitcnt vmcnt(0)\n"
      "s_sub_u32 s57, s57, 1\n"
      "s_cbranch_scc0 .Lq_q1\n"
      "s_mov_b32 s57, 0\n"
      "s_branch .Lq_q1x\n"
      ".Lq_q2r:\n"
      "buffer_load_dwordx4 v[220:223], v1, s[24:27], s54 offen offset:2048 sc1\n"
      "buffer_load_dwordx4 v[224:227], v1, s[24:27], s54 offen offset:3072 sc1\n"
      "buffer_load_dwordx4 v[228:231], v1, s[24:27], s55 offen offset:0 sc1\n"
      "buffer_load_dwordx4 v[232:235], v1, s[24:27], s55 offen offset:1024 sc1\n"
      "buffer_load_dwordx4 v[236:239], v1, s[24:27], s55 offen offset:2048 sc1\n"
      "s_waitcnt vmcnt(0)\n"
      "s_sub_u32 s57, s57, 1\n"
      "s_cbranch_scc0 .Lq_q2\n"
      "s_mov_b32 s57, 0\n"
      "s_branch .Lq_q2x\n"
      ".Lq_q3r:\n"
      "buffer_load_dwordx4 v[240:243], v1, s[24:27], s55 offen offset:3072 sc1\n"
      "buffer_load_dwordx4 v[244:247], v1, s[24:27], s56 offen offset:0 sc1\n"
      "buffer_load_dwordx4 v[248:251], v1, s[24:27], s56 offen offset:1024 sc1\n"
      "buffer_load_dwordx4 v[252:255], v1, s[24:27], s56 offen offset:2048 sc1\n"
      "s_waitcnt vmcnt(0)\n"
      "s_sub_u32 s57, s57, 1\n"
      "s_cbranch_scc0 .Lq_q3\n"
      "s_mov_b32 s57, 0\n"
      "s_branch .Lq_q3x\n"
      ".Lq_exr:\n"
      "buffer_load_dwordx4 v[80:83], v1, s[24:27], s49 offen offset:0 sc1\n"
      "buffer_load_dwordx4 v[84:87], v1, s[24:27], s49 offen offset:1024 sc1\n"
      "buffer_load_dwordx4 v[88:91], v1, s[24:27], s49 offen offset:2048 sc1\n"
      "s_waitcnt vmcnt(0)\n"
      "s_sub_u32 s57, s57, 1\n"
      "s_cbranch_scc0 .Lq_ex\n"
      "s_mov_b32 s57, 0\n"
      "s_branch .Lq_exx\n"
      ".Lq_fin:\n"
      "s_endpgm\n"
      ".Lq_end:\n"
      :
      : "{s[0:1]}"(__builtin_amdgcn_kernarg_segment_ptr()), "{s2}"(blockIdx.x), "{v0}"(threadIdx.x)
      : "v255", "s63", "vcc", "scc", "memory");
}

extern "C" void kernel_launch(void* const* d_in, const int* in_sizes, int n_in, void* d_out, int out_size, void* d_ws,
                              size_t ws_size, hipStream_t stream) {
  (void)hipFuncSetAttribute((const void*)kmain, hipFuncAttributeMaxDynamicSharedMemorySize, 161856);
  kmain<<<256, 512, 161856, stream>>>(d_in[0], d_in[1], d_in[2], d_in[3], d_in[4], d_in[5], d_out, d_ws);
}
```
